```python
import torch

def _softmax(x, axis=-1):
    return torch.softmax(x, dim=axis)
def _gelu(x):
    return torch.nn.functional.gelu(x, approximate="tanh")
def _silu(x):
    return torch.nn.functional.silu(x)
def _sigmoid(x):
    return torch.sigmoid(x)
def _layernorm(x, axis=-1, eps=1e-5):
    m = x.mean(dim=axis, keepdim=True)
    v = x.var(dim=axis, keepdim=True, unbiased=False)
    return (x - m) / torch.sqrt(v + eps)
def _scatter_add(tgt, idx, src, axis=0):
    return tgt.clone().index_add_(axis, idx, src)
def _segment_sum(src, seg, n):
    out = torch.zeros((n,) + src.shape[1:], dtype=src.dtype)
    return out.index_add_(0, seg, src)
def _pad(x, pw):
    flat = tuple(v for pair in reversed(pw) for v in pair)
    return torch.nn.functional.pad(x, flat)

kernel_name = 'gated_mix__sliding_local_dilated__mixer_token_mlp__prompt'

def setup_inputs():

    _w = (lambda a: (lambda t: t.to(torch.bfloat16) if t.is_floating_point() else t)(torch.as_tensor(a)))
    def _A_setup():
        B, S, D = 1, 2048, 1024
        return {
            "x": _w((lambda *s: torch.randn(*s, dtype=torch.bfloat16) * 0.05)(B, S, D)),
            "wqkv": _w((lambda *s: torch.randn(*s, dtype=torch.bfloat16) * 0.8)(D, 3 * D)),
            "wo": _w((lambda *s: torch.randn(*s, dtype=torch.bfloat16) * 0.05)(D, D)),
        }
    _A_in = _A_setup()

    def _B_setup():
        B, S, D, F = 1, 2048, 1024, 3584
        return {
            "x": _w((lambda *s: torch.randn(*s, dtype=torch.bfloat16) * 0.05)(B, S, D)),
            "w1": _w((lambda *s: torch.randn(*s, dtype=torch.bfloat16) * 3.2)(S, F) * (S ** -0.5)),
            "b1": _w((lambda *s: torch.randn(*s, dtype=torch.bfloat16) * 3.2)(F) * 0.02),
            "w2": _w((lambda *s: torch.randn(*s, dtype=torch.bfloat16) * 0.2)(F, S) * (F ** -0.5)),
            "b2": _w((lambda *s: torch.randn(*s, dtype=torch.bfloat16) * 0.2)(S) * 0.02),
        }
    _B_in = _B_setup()

    return {
        "A_x": _A_in["x"],
        "A_wqkv": _A_in["wqkv"],
        "A_wo": _A_in["wo"],
        "B_x": _B_in["x"],
        "B_w1": _B_in["w1"],
        "B_b1": _B_in["b1"],
        "B_w2": _B_in["w2"],
        "B_b2": _B_in["b2"],
        "gate_w": _w((lambda *s: torch.randn(*s, dtype=torch.bfloat16) * 0.05)(1024) * 16.0),
    }

def reference(A_x, A_wqkv, A_wo, B_x, B_w1, B_b1, B_w2, B_b2, gate_w):
    def _A_ref(x, wqkv, wo):
        B, S, D = x.shape
        H = 32; Dh = D // H
        W, dil = 8, 2
        qkv = (lambda x, *d: x.permute(*d))((x @ wqkv).reshape(B, S, 3, H, Dh), 2, 0, 3, 1, 4)
        q, k, v = qkv[0], qkv[1], qkv[2]
        scale = Dh ** -0.5
        pos = torch.arange(S)
        off = torch.arange(W) * dil
        idx = pos[:, None] - off[None, :]
        mask = idx >= 0
        idx_c = torch.where(mask, idx, 0).reshape(-1)
        kg = k[:, :, idx_c, :].reshape(B, H, S, W, Dh)
        vg = v[:, :, idx_c, :].reshape(B, H, S, W, Dh)
        logits = torch.einsum("bhsd,bhswd->bhsw", q, kg) * scale
        logits = torch.where(mask[None, None, :, :], logits, -1e9)
        attn = _softmax(logits, axis=-1)
        o = torch.einsum("bhsw,bhswd->bhsd", attn, vg)
        return (lambda x, *d: x.permute(*d))(o, 0, 2, 1, 3).reshape(B, S, D) @ wo

    def _B_ref(x, w1, b1, w2, b2):
        B, S, D = x.shape
        h = _layernorm(x, axis=-1)
        u = torch.einsum("bsd,sf->bdf", h, w1) + b1
        u = _gelu(u)
        y = torch.einsum("bdf,fs->bds", u, w2) + b2
        return (lambda x, *d: x.permute(*d))(y, 0, 2, 1)

    x = A_x
    g = _sigmoid((x * gate_w).sum(-1, keepdims=True))
    a = _A_ref(x, A_wqkv, A_wo)
    b = _B_ref(x, B_w1, B_b1, B_w2, B_b2)
    return g * a + (1 - g) * b
```

```cpp
#include <hip/hip_runtime.h>
#include <hip/hip_bf16.h>

typedef __attribute__((ext_vector_type(2))) __bf16 bf16x2_t;
typedef __attribute__((ext_vector_type(8))) __bf16 bf16x8_t;
typedef __attribute__((ext_vector_type(4))) short s16x4;
typedef __attribute__((ext_vector_type(8))) short s16x8;
typedef __attribute__((ext_vector_type(4))) float f32x4;
typedef __attribute__((ext_vector_type(2))) float f32x2;
typedef __attribute__((ext_vector_type(4))) unsigned u32x4;
typedef __attribute__((ext_vector_type(2))) unsigned u32x2;
typedef __attribute__((address_space(3))) char* lds_ptr;

#define DI __device__ __forceinline__

constexpr int S_ = 2048, D_ = 1024, F_ = 3584;

constexpr size_t WS_XT = 0;
constexpr size_t WS_HT = WS_XT + (size_t)S_ * D_ * 2;
constexpr size_t WS_OT = WS_HT + (size_t)S_ * D_ * 2;
constexpr size_t WS_UT = WS_OT + (size_t)S_ * D_ * 2;
constexpr size_t WS_G = WS_UT + (size_t)D_ * F_ * 2;

DI unsigned pack2(float a, float b) {
  f32x2 f = {a, b};
  return __builtin_bit_cast(unsigned, __builtin_convertvector(f, bf16x2_t));
}
DI float bf_lo(unsigned u) { return __builtin_bit_cast(float, u << 16); }
DI float bf_hi(unsigned u) { return __builtin_bit_cast(float, u & 0xffff0000u); }

DI float dot2(unsigned a, unsigned b, float c) {
  return __builtin_amdgcn_fdot2_f32_bf16(__builtin_bit_cast(bf16x2_t, a), __builtin_bit_cast(bf16x2_t, b), c, false);
}

DI void glds16(unsigned lds_dst, unsigned voff, const void* sbase) {
  asm volatile("s_mov_b32 m0, %0\n\ts_nop 0\n\tglobal_load_lds_dwordx4 %1, %2" ::"s"(lds_dst), "v"(voff), "s"(sbase)
               : "memory");
}
template <int N>
DI void wait_vm() {
  asm volatile("s_waitcnt vmcnt(%0)" ::"i"(N) : "memory");
}
DI void bar_raw() { asm volatile("s_barrier" ::: "memory"); }
DI void bar_lgkm() { asm volatile("s_waitcnt lgkmcnt(0)\n\ts_barrier" ::: "memory"); }

DI bf16x8_t lds_b128(unsigned addr) {
  return *(const __attribute__((address_space(3))) bf16x8_t*)(size_t)addr;
}
DI bf16x8_t lds_tr(unsigned addr, unsigned off2) {
  s16x4 lo = __builtin_amdgcn_ds_read_tr16_b64_v4i16((__attribute__((address_space(3))) s16x4*)(size_t)addr);
  s16x4 hi = __builtin_amdgcn_ds_read_tr16_b64_v4i16((__attribute__((address_space(3))) s16x4*)(size_t)(addr + off2));
  s16x8 r = __builtin_shufflevector(lo, hi, 0, 1, 2, 3, 4, 5, 6, 7);
  return __builtin_bit_cast(bf16x8_t, r);
}
DI f32x4 mfma(bf16x8_t a, bf16x8_t b, f32x4 c) { return __builtin_amdgcn_mfma_f32_16x16x32_bf16(a, b, c, 0, 0, 0); }

template <int N, int KMAX>
DI void wait_vm_k(int k) {
  if (k >= KMAX) wait_vm<N * KMAX>();
  else if constexpr (KMAX > 0) wait_vm_k<N, KMAX - 1>(k);
}

__global__ __launch_bounds__(128) void k0_prep(const u32x4* __restrict__ x, const u32x4* __restrict__ gw, char* __restrict__ ws) {
  __shared__ float red[2][24];
  const int t = threadIdx.x, lane = t & 63, wv = t >> 6;
  const int s0 = blockIdx.x * 8;
  u32x4 raw[8];
#pragma unroll
  for (int r = 0; r < 8; ++r) raw[r] = x[(size_t)(s0 + r) * 128 + t];
  u32x4 gwv = gw[t];
  {
    const int kb = t >> 2, q = t & 3, rb = s0 >> 4;
    u32x4* dst = (u32x4*)(ws + WS_XT) + ((size_t)(kb * 128 + rb) * 64 + q * 16 + (s0 & 15));
#pragma unroll
    for (int r = 0; r < 8; ++r) dst[r] = raw[r];
  }
  float v[8][8], gwf[8];
#pragma unroll
  for (int i = 0; i < 4; ++i) { gwf[2 * i] = bf_lo(gwv[i]); gwf[2 * i + 1] = bf_hi(gwv[i]); }
  float sm[8], gd[8];
#pragma unroll
  for (int r = 0; r < 8; ++r) {
    float a = 0.f, b = 0.f;
#pragma unroll
    for (int i = 0; i < 4; ++i) { v[r][2 * i] = bf_lo(raw[r][i]); v[r][2 * i + 1] = bf_hi(raw[r][i]); }
#pragma unroll
    for (int i = 0; i < 8; ++i) { a += v[r][i]; b += v[r][i] * gwf[i]; }
    sm[r] = a; gd[r] = b;
  }
#pragma unroll
  for (int m = 1; m < 64; m <<= 1) {
#pragma unroll
    for (int r = 0; r < 8; ++r) { sm[r] += __shfl_xor(sm[r], m); gd[r] += __shfl_xor(gd[r], m); }
  }
  if (lane == 0) {
#pragma unroll
    for (int r = 0; r < 8; ++r) { red[wv][r] = sm[r]; red[wv][8 + r] = gd[r]; }
  }
  __syncthreads();
  float vr[8];
#pragma unroll
  for (int r = 0; r < 8; ++r) {
    const float mean = (red[0][r] + red[1][r]) * (1.0f / D_);
    float a = 0.f;
#pragma unroll
    for (int i = 0; i < 8; ++i) { v[r][i] -= mean; a += v[r][i] * v[r][i]; }
    vr[r] = a;
  }
  if (t < 8) {
    const float z = red[0][8 + t] + red[1][8 + t];
    ((float*)(ws + WS_G))[s0 + t] = 1.0f / (1.0f + __expf(-z));
  }
#pragma unroll
  for (int m = 1; m < 64; m <<= 1) {
#pragma unroll
    for (int r = 0; r < 8; ++r) vr[r] += __shfl_xor(vr[r], m);
  }
  if (lane == 0) {
#pragma unroll
    for (int r = 0; r < 8; ++r) red[wv][16 + r] = vr[r];
  }
  __syncthreads();
  float rs[8];
#pragma unroll
  for (int r = 0; r < 8; ++r) rs[r] = rsqrtf((red[0][16 + r] + red[1][16 + r]) * (1.0f / D_) + 1e-5f);
  {
    const int kb = s0 >> 5, q = (s0 & 31) >> 3, rb = t >> 1;
    u32x4* dst = (u32x4*)(ws + WS_HT) + ((size_t)(kb * 64 + rb) * 64 + q * 16 + (t & 1) * 8);
#pragma unroll
    for (int i = 0; i < 8; ++i) {
      u32x4 o;
#pragma unroll
      for (int p = 0; p < 4; ++p) o[p] = pack2(v[2 * p][i] * rs[2 * p], v[2 * p + 1][i] * rs[2 * p + 1]);
      dst[i] = o;
    }
  }
}

DI int sw_std(int r) { return ((r >> 1) & 1) | (((r >> 3) & 1) << 1); }
DI int sw_prm(int r) { return (r >> 1) & 3; }

constexpr int G1_STAGE = 29 * 1024, G1_NS = 5, G1_T = 32;
constexpr int G3_STAGE = 22 * 1024, G3_NS = 6, G3_T = 64;
constexpr int K1_LDS = G1_STAGE * G1_NS;
static_assert(G3_STAGE * G3_NS <= K1_LDS, "");

struct G1Frags { bf16x8_t a[4], ah, b[6], bh; };

DI void g1_read(G1Frags& f, unsigned so, unsigned a_base, unsigned ah_base, unsigned b0, unsigned b1, unsigned bh) {
#pragma unroll
  for (int p = 0; p < 3; ++p) {
    f.b[2 * p] = lds_tr(b0 + so + p * 4096, 512);
    f.b[2 * p + 1] = lds_tr(b1 + so + p * 4096, 512);
  }
#pragma unroll
  for (int m = 0; m < 4; ++m) f.a[m] = lds_b128(a_base + so + m * 1024);
  f.bh = lds_tr(bh + so, 512);
  f.ah = lds_b128(ah_base + so);
}
DI void g1_mma(f32x4 (&acc)[4][6], f32x4& acch, const G1Frags& f) {
#pragma unroll
  for (int m = 0; m < 4; ++m)
#pragma unroll
    for (int n = 0; n < 6; ++n) acc[m][n] = mfma(f.b[n], f.a[m], acc[m][n]);
  acch = mfma(f.bh, f.ah, acch);
}
template <int NMFMA, int NDS>
DI void interleave() {
#pragma unroll
  for (int i = 0; i < NMFMA; ++i) {
    __builtin_amdgcn_sched_group_barrier(0x008, 1, 0);
    if (i < NDS) __builtin_amdgcn_sched_group_barrier(0x100, 1, 0);
    __builtin_amdgcn_sched_group_barrier(0x002, 1, 0);
  }
}

DI void gemm1_attn(int tile_m, int tile_n, const char* __restrict__ wqkv, char* __restrict__ ws) {
  const int tid = threadIdx.x, lane = tid & 63;
  const int w = __builtin_amdgcn_readfirstlane(tid >> 6);
  const int wm = w >> 1, wn = w & 1;
  const char* srcA = ws + WS_XT;
  const char* srcB = wqkv + (size_t)tile_n * 128;
  const unsigned voffA = (unsigned)(tile_m * 16 + 2 * w) * 1024 + lane * 16;
  const unsigned voffH = (unsigned)(tile_m > 0 ? tile_m * 16 - 1 : 0) * 1024 + lane * 16;
  auto voffB = [&](int n) {
    const int r = (n & 3) * 8 + (lane >> 3), c = (lane & 7) ^ (sw_std(r) << 1);
    return (unsigned)(r * (3 * D_ * 2) + (n >> 2) * (D_ * 2) + c * 16);
  };
  const unsigned voffB0 = voffB(w);
  const unsigned voffX = (w < 4) ? voffB(8 + w) : voffH;
  auto issue = [&](int st, unsigned slot) {
    const char* a = srcA + (size_t)st * (128 * 1024);
    const char* b = srcB + (size_t)st * (32 * 3 * D_ * 2);
    glds16(slot + 2 * w * 1024, voffA, a);
    glds16(slot + 2 * w * 1024 + 1024, voffA + 1024, a);
    glds16(slot + 17 * 1024 + w * 1024, voffB0, b);
    if (w < 4) glds16(slot + 17 * 1024 + (8 + w) * 1024, voffX, b);
    else if (w == 4) glds16(slot + 16 * 1024, voffX, a);
  };
  auto waitk = [&](int k) {
    if (w <= 4) wait_vm_k<4, G1_NS - 1>(k);
    else wait_vm_k<3, G1_NS - 1>(k);
  };
  const unsigned a_base = wm * 4096 + lane * 16;
  const unsigned ah_base = 16 * 1024 + lane * 16;
  const int trr = (lane >> 4) * 8 + ((lane & 15) >> 2), trp = lane & 3;
  auto tr_addr = [&](int jb) { return (unsigned)(17 * 1024 + trr * 128 + ((jb ^ sw_std(trr)) * 32) + trp * 8); };
  const unsigned b0 = tr_addr(2 * wn), b1 = tr_addr(2 * wn + 1);
  const unsigned bh = tr_addr(2 * wn + (wm & 1)) + (1 + (wm >> 1)) * 4096;

  f32x4 acc[4][6], acch = {0.f, 0.f, 0.f, 0.f};
#pragma unroll
  for (int m = 0; m < 4; ++m)
#pragma unroll
    for (int n = 0; n < 6; ++n) acc[m][n] = f32x4{0.f, 0.f, 0.f, 0.f};

#pragma unroll
  for (int s = 0; s < G1_NS; ++s) issue(s, s * G1_STAGE);
  waitk(G1_NS - 1);
  bar_raw();
  G1Frags fx, fy;
  g1_read(fx, 0, a_base, ah_base, b0, b1, bh);
  unsigned cur = 0;
  auto step = [&](int t, G1Frags& fc, G1Frags& fn) {
    waitk(min(G1_NS - 2, G1_T - 2 - t));
    bar_lgkm();
    if (t + G1_NS < G1_T) issue(t + G1_NS, cur);
    cur += G1_STAGE;
    if (cur == G1_NS * G1_STAGE) cur = 0;
    __builtin_amdgcn_sched_barrier(0);
    g1_read(fn, cur, a_base, ah_base, b0, b1, bh);
    g1_mma(acc, acch, fc);
    interleave<25, 19>();
    __builtin_amdgcn_sched_barrier(0);
  };
  for (int t = 0; t < G1_T - 2; t += 2) {
    step(t, fx, fy);
    step(t + 1, fy, fx);
  }
  step(G1_T - 2, fx, fy);
  g1_mma(acc, acch, fy);
  bar_lgkm();

  constexpr int PL = 272 * 16;
  {
    const unsigned wbase = wn * (12 * PL) + (lane >> 5) * PL + ((lane >> 4) & 1) * 8 + (lane & 15) * 16;
#pragma unroll
    for (int m = 0; m < 4; ++m)
#pragma unroll
      for (int n = 0; n < 6; ++n) {
        u32x2 o = {pack2(acc[m][n][0], acc[m][n][1]), pack2(acc[m][n][2], acc[m][n][3])};
        *(__attribute__((address_space(3))) u32x2*)(size_t)(wbase + n * 2 * PL + (16 + wm * 64 + m * 16) * 16) = o;
      }
    u32x2 o = {pack2(acch[0], acch[1]), pack2(acch[2], acch[3])};
    *(__attribute__((address_space(3))) u32x2*)(size_t)(wbase + (2 + wm) * 2 * PL) = o;
  }
  const int hd = tid >> 8, row = tid & 255, s = tile_m * 256 + row;
  const float gate = ((const float*)(ws + WS_G))[s];
  bar_lgkm();
  {
    const unsigned rbase = hd * (12 * PL) + (16 + row) * 16;
    auto ld = [&](int mat, int c, int back) {
      return *(const __attribute__((address_space(3))) u32x4*)(size_t)(rbase + (mat * 4 + c) * PL - back * 32);
    };
    u32x4 q[4];
#pragma unroll
    for (int c = 0; c < 4; ++c) q[c] = ld(0, c, 0);
    float lg[8];
#pragma unroll
    for (int wd = 0; wd < 8; ++wd) {
      float a = 0.f;
#pragma unroll
      for (int c = 0; c < 4; ++c) {
        u32x4 kk = ld(1, c, wd);
#pragma unroll
        for (int i = 0; i < 4; ++i)
          a = dot2(q[c][i], kk[i], a);
      }
      lg[wd] = (s - 2 * wd >= 0) ? a * 0.17677669529663687f : -1e9f;
    }
    float mx = lg[0];
#pragma unroll
    for (int wd = 1; wd < 8; ++wd) mx = fmaxf(mx, lg[wd]);
    float sum = 0.f;
#pragma unroll
    for (int wd = 0; wd < 8; ++wd) { lg[wd] = __expf(lg[wd] - mx); sum += lg[wd]; }
    const float sc = gate / sum;
    float o[32];
#pragma unroll
    for (int i = 0; i < 32; ++i) o[i] = 0.f;
#pragma unroll
    for (int wd = 0; wd < 8; ++wd) {
      const bool ok = (s - 2 * wd >= 0);
      const float p = lg[wd] * sc;
#pragma unroll
      for (int c = 0; c < 4; ++c) {
        u32x4 vv = ld(2, c, ok ? wd : 0);
#pragma unroll
        for (int i = 0; i < 4; ++i) {
          o[c * 8 + 2 * i] += p * bf_lo(vv[i]);
          o[c * 8 + 2 * i + 1] += p * bf_hi(vv[i]);
        }
      }
    }
    u32x4* dst = (u32x4*)(ws + WS_OT) + ((size_t)((tile_n * 2 + hd) * 128 + (s >> 4)) * 64 + (s & 15));
#pragma unroll
    for (int qq = 0; qq < 4; ++qq) {
      u32x4 r = {pack2(o[4 * qq], o[4 * qq + 1]), pack2(o[4 * qq + 2], o[4 * qq + 3]),
                 pack2(o[16 + 4 * qq], o[16 + 4 * qq + 1]), pack2(o[16 + 4 * qq + 2], o[16 + 4 * qq + 3])};
      dst[qq * 16] = r;
    }
  }
}

struct G3Frags { bf16x8_t h[4], w[7]; };

DI float gelu_tanh(float u) {
  const float c0 = -2.0f * 0.7978845608028654f * 1.4426950408889634f;
  const float c1 = c0 * 0.044715f;
  const float e = __builtin_amdgcn_exp2f(u * __builtin_fmaf(u * u, c1, c0));
  return u * __builtin_amdgcn_rcpf(1.0f + e);
}

DI void gemm3_gelu(int tile_d, int tile_f, const char* __restrict__ w1, const char* __restrict__ b1, char* __restrict__ ws) {
  const int tid = threadIdx.x, lane = tid & 63;
  const int w = __builtin_amdgcn_readfirstlane(tid >> 6);
  const int odd = tile_f & 1;
  if (w >= 4) {
    const int lw = w - 4;
    const char* srcA = ws + WS_HT;
    const char* srcB = w1 + (size_t)tile_f * 448;
    const unsigned voffA = (unsigned)(tile_d * 8 + 2 * lw) * 1024 + lane * 16;
    const int r = lw * 8 + (lane >> 3), c = (lane & 7) ^ (sw_std(r) << 1);
    const unsigned voffB = (unsigned)(r * (F_ * 2) + odd * 64 + c * 16);
    const int hg = (lw & 1) * 2 + ((lane >> 4) & 1), hr = hg * 8 + (lane >> 5) * 4 + ((lane >> 2) & 3);
    const int hc = (lane & 3) ^ ((hg & 1) << 1);
    const unsigned voffH = (unsigned)(hr * (F_ * 2) + (odd ? 0 : 384) + hc * 16);
    auto issue = [&](int st, unsigned slot) {
      const char* a = srcA + (size_t)st * (64 * 1024);
      const char* b = srcB + (size_t)st * (32 * F_ * 2);
      glds16(slot + 2 * lw * 1024, voffA, a);
      glds16(slot + 2 * lw * 1024 + 1024, voffA + 1024, a);
      glds16(slot + 8192 + lw * 1024, voffB, b);
      glds16(slot + 8192 + 4096 + lw * 1024, voffB + 128, b);
      glds16(slot + 8192 + 8192 + lw * 1024, voffB + 256, b);
      if (lw < 2) glds16(slot + 8192 + 12288 + lw * 1024, voffH, b);
    };
    auto waitk = [&](int k) {
      if (lw < 2) wait_vm_k<6, G3_NS - 1>(k);
      else wait_vm_k<5, G3_NS - 1>(k);
    };
#pragma unroll
    for (int s = 0; s < G3_NS; ++s) issue(s, s * G3_STAGE);
    waitk(G3_NS - 1);
    bar_raw();
    unsigned cur = 0;
    for (int t = 0; t < G3_T - 1; ++t) {
      waitk(min(G3_NS - 2, G3_T - 2 - t));
      bar_raw();
      if (t + G3_NS < G3_T) issue(t + G3_NS, cur);
      cur += G3_STAGE;
      if (cur == G3_NS * G3_STAGE) cur = 0;
    }
    return;
  }
  const int wd = w >> 1, wf = w & 1;
  const unsigned h_base = wd * 4096 + lane * 16;
  const int g = lane >> 4, qq = (lane & 15) >> 2, trp = lane & 3, trr = g * 8 + qq;
  unsigned wb[7];
  int fcol[7];
#pragma unroll
  for (int i = 0; i < 7; ++i) {
    const int lt = wf * 7 + i;
    const unsigned pa = 8192 + (lt >> 2) * 4096 + trr * 128 + (((lt & 3) ^ sw_std(trr)) * 32) + trp * 8;
    const unsigned ha = 8192 + 12288 + (g >> 1) * 1024 + (g & 1) * 256 + qq * 64 + (((lt & 1) ^ (g & 1)) * 32) + trp * 8;
    wb[i] = lt < 12 ? pa : ha;
    fcol[i] = tile_f * 224 + (odd ? (lt < 12 ? 32 + 16 * lt : 16 * (lt - 12)) : 16 * lt);
  }
  u32x2 bias[7];
#pragma unroll
  for (int i = 0; i < 7; ++i) bias[i] = *(const u32x2*)(b1 + (size_t)(fcol[i] + 4 * g) * 2);

  f32x4 acc[7][4];
#pragma unroll
  for (int i = 0; i < 7; ++i)
#pragma unroll
    for (int m = 0; m < 4; ++m) acc[i][m] = f32x4{0.f, 0.f, 0.f, 0.f};
  auto rd = [&](G3Frags& f, unsigned so) {
#pragma unroll
    for (int m = 0; m < 4; ++m) f.h[m] = lds_b128(h_base + so + m * 1024);
#pragma unroll
    for (int i = 0; i < 7; ++i) f.w[i] = lds_tr(wb[i] + so, 512);
  };
  auto mm = [&](const G3Frags& f) {
#pragma unroll
    for (int i = 0; i < 7; ++i)
#pragma unroll
      for (int m = 0; m < 4; ++m) acc[i][m] = mfma(f.w[i], f.h[m], acc[i][m]);
  };
  G3Frags fx, fy;
  bar_raw();
  rd(fx, 0);
  unsigned cur = 0;
  auto step = [&](G3Frags& fc, G3Frags& fn) {
    bar_lgkm();
    cur += G3_STAGE;
    if (cur == G3_NS * G3_STAGE) cur = 0;
    __builtin_amdgcn_sched_barrier(0);
    rd(fn, cur);
    mm(fc);
    interleave<28, 18>();
    __builtin_amdgcn_sched_barrier(0);
  };
  for (int t = 0; t < G3_T - 2; t += 2) {
    step(fx, fy);
    step(fy, fx);
  }
  step(fx, fy);
  mm(fy);
  char* ut = ws + WS_UT;
#pragma unroll
  for (int m = 0; m < 4; ++m) {
    u32x2 pk[7];
#pragma unroll
    for (int i = 0; i < 7; ++i) {
      const float v0 = gelu_tanh(acc[i][m][0] + bf_lo(bias[i][0])), v1 = gelu_tanh(acc[i][m][1] + bf_hi(bias[i][0]));
      const float v2 = gelu_tanh(acc[i][m][2] + bf_lo(bias[i][1])), v3 = gelu_tanh(acc[i][m][3] + bf_hi(bias[i][1]));
      pk[i] = u32x2{pack2(v0, v1), pack2(v2, v3)};
    }
    const size_t rbo = (size_t)(tile_d * 8 + wd * 4 + m) * 1024 + lane * 16;
    if (wf == 0) {
#pragma unroll
      for (int p = 0; p < 3; ++p)
        *(u32x4*)(ut + (size_t)(fcol[2 * p] >> 5) * 65536 + rbo) = u32x4{pk[2 * p][0], pk[2 * p][1], pk[2 * p + 1][0], pk[2 * p + 1][1]};
      *(u32x2*)(ut + (size_t)(fcol[6] >> 5) * 65536 + rbo) = pk[6];
    } else {
      *(u32x2*)(ut + (size_t)(fcol[0] >> 5) * 65536 + rbo + 8) = pk[0];
#pragma unroll
      for (int p = 0; p < 3; ++p)
        *(u32x4*)(ut + (size_t)(fcol[2 * p + 1] >> 5) * 65536 + rbo) =
            u32x4{pk[2 * p + 1][0], pk[2 * p + 1][1], pk[2 * p + 2][0], pk[2 * p + 2][1]};
    }
  }
}

__global__ __launch_bounds__(512, 2) void k1_gemms(const char* __restrict__ wqkv, const char* __restrict__ w1,
                                                   const char* __restrict__ b1, char* __restrict__ ws) {
  const int b = blockIdx.x, xcd = b & 7, j = b >> 3;
  if (xcd < 4) gemm1_attn((xcd & 1) * 4 + (j & 3), (xcd >> 1) * 8 + (j >> 2), wqkv, ws);
  else gemm3_gelu(j & 7, (xcd - 4) * 4 + (j >> 3), w1, b1, ws);
}

constexpr int K2_STAGE = 24 * 1024, K2_NS = 6, K2_T1 = 56, K2_T = 72;
constexpr int K2_LDS = K2_STAGE * K2_NS;

struct K2Frags { bf16x8_t s[2][4], d[2][2]; };

__global__ __launch_bounds__(512, 2) void k2_out(const char* __restrict__ wo, const char* __restrict__ w2,
                                                 const char* __restrict__ b2, const char* __restrict__ ws,
                                                 char* __restrict__ out) {
  const int b = blockIdx.x, xcd = b & 7, j = b >> 3;
  const int tile_s = (xcd >> 1) * 4 + (j & 3), tile_d = (xcd & 1) * 8 + (j >> 2);
  const int tid = threadIdx.x, lane = tid & 63;
  const int w = __builtin_amdgcn_readfirstlane(tid >> 6);
  if (w >= 4) {
    const int lw = w - 4;
    const int r = lw * 8 + (lane >> 3), c = (lane & 7) ^ (sw_prm(r) << 1);
    const unsigned voffW2 = (unsigned)(r * (S_ * 2) + tile_s * 256 + c * 16);
    const unsigned voffU = (unsigned)(tile_d * 4 + lw) * 1024 + lane * 16;
    const unsigned voffO = (unsigned)(tile_s * 8 + 2 * lw) * 1024 + lane * 16;
    const unsigned voffWo = (unsigned)(r * (D_ * 2) + tile_d * 128 + c * 16);
    auto issue = [&](int st, unsigned slot) {
      if (st < K2_T1) {
        const char* a = w2 + (size_t)st * (64 * S_ * 2);
        const char* u = ws + WS_UT + (size_t)st * (2 * 65536);
#pragma unroll
        for (int kk = 0; kk < 2; ++kk) {
          glds16(slot + kk * 8192 + lw * 1024, voffW2 + kk * (32 * S_ * 2), a);
          glds16(slot + kk * 8192 + 4096 + lw * 1024, voffW2 + kk * (32 * S_ * 2) + 128, a);
          glds16(slot + 16384 + kk * 4096 + lw * 1024, voffU + kk * 65536, u);
        }
      } else {
        const char* o = ws + WS_OT + (size_t)(st - K2_T1) * (2 * 131072);
        const char* a = wo + (size_t)(st - K2_T1) * (64 * D_ * 2);
#pragma unroll
        for (int kk = 0; kk < 2; ++kk) {
          glds16(slot + kk * 8192 + 2 * lw * 1024, voffO + kk * 131072, o);
          glds16(slot + kk * 8192 + 2 * lw * 1024 + 1024, voffO + kk * 131072 + 1024, o);
          glds16(slot + 16384 + kk * 4096 + lw * 1024, voffWo + kk * (32 * D_ * 2), a);
        }
      }
    };
#pragma unroll
    for (int s = 0; s < K2_NS; ++s) issue(s, s * K2_STAGE);
    wait_vm<6 * (K2_NS - 1)>();
    bar_raw();
    unsigned cur = 0;
    for (int t = 0; t < K2_T - 1; ++t) {
      wait_vm_k<6, K2_NS - 2>(min(K2_NS - 2, K2_T - 2 - t));
      bar_raw();
      if (t + K2_NS < K2_T) issue(t + K2_NS, cur);
      cur += K2_STAGE;
      if (cur == K2_NS * K2_STAGE) cur = 0;
    }
    return;
  }
  const int ws_ = w >> 1, wd = w & 1;
  const int g = lane >> 4, trr = g * 4 + ((lane & 15) >> 2), trp = lane & 3;
  unsigned ta[4];
#pragma unroll
  for (int i = 0; i < 4; ++i) ta[i] = trr * 128 + ((i ^ sw_prm(trr)) * 32) + trp * 8;
  const unsigned tw0 = trr * 128 + (((2 * wd) ^ sw_prm(trr)) * 32) + trp * 8 + 16384;
  const unsigned tw1 = trr * 128 + (((2 * wd + 1) ^ sw_prm(trr)) * 32) + trp * 8 + 16384;
  const unsigned lin = lane * 16;
  float gt[4], bb[4];
#pragma unroll
  for (int i = 0; i < 4; ++i) {
    const int s = tile_s * 128 + ws_ * 64 + i * 16 + (lane & 15);
    gt[i] = ((const float*)(ws + WS_G))[s];
    bb[i] = bf_lo(((const unsigned short*)b2)[s]);
  }
  f32x4 acc[4][2];
#pragma unroll
  for (int i = 0; i < 4; ++i)
#pragma unroll
    for (int n = 0; n < 2; ++n) acc[i][n] = f32x4{0.f, 0.f, 0.f, 0.f};
  auto rd1 = [&](K2Frags& f, unsigned so) {
#pragma unroll
    for (int kk = 0; kk < 2; ++kk) {
#pragma unroll
      for (int n = 0; n < 2; ++n) f.d[kk][n] = lds_b128(lin + so + 16384 + kk * 4096 + (wd * 2 + n) * 1024);
#pragma unroll
      for (int i = 0; i < 4; ++i) f.s[kk][i] = lds_tr(ta[i] + so + kk * 8192 + ws_ * 4096, 2048);
    }
  };
  auto rd2 = [&](K2Frags& f, unsigned so) {
#pragma unroll
    for (int kk = 0; kk < 2; ++kk) {
      f.d[kk][0] = lds_tr(tw0 + so + kk * 4096, 2048);
      f.d[kk][1] = lds_tr(tw1 + so + kk * 4096, 2048);
#pragma unroll
      for (int i = 0; i < 4; ++i) f.s[kk][i] = lds_b128(lin + so + kk * 8192 + (ws_ * 4 + i) * 1024);
    }
  };
  auto mm = [&](const K2Frags& f) {
#pragma unroll
    for (int kk = 0; kk < 2; ++kk)
#pragma unroll
      for (int i = 0; i < 4; ++i)
#pragma unroll
        for (int n = 0; n < 2; ++n) acc[i][n] = mfma(f.d[kk][n], f.s[kk][i], acc[i][n]);
  };
  K2Frags fx, fy;
  bar_raw();
  rd1(fx, 0);
  unsigned cur = 0;
  auto adv = [&]() {
    bar_lgkm();
    cur += K2_STAGE;
    if (cur == K2_NS * K2_STAGE) cur = 0;
    __builtin_amdgcn_sched_barrier(0);
  };
  auto step1 = [&](K2Frags& fc, K2Frags& fn) { adv(); rd1(fn, cur); mm(fc); __builtin_amdgcn_sched_barrier(0); };
  auto step2 = [&](K2Frags& fc, K2Frags& fn) { adv(); rd2(fn, cur); mm(fc); __builtin_amdgcn_sched_barrier(0); };
  for (int t = 0; t < K2_T1 - 2; t += 2) {
    step1(fx, fy);
    step1(fy, fx);
  }
  step1(fx, fy);
  step2(fy, fx);
#pragma unroll
  for (int i = 0; i < 4; ++i)
#pragma unroll
    for (int n = 0; n < 2; ++n)
#pragma unroll
      for (int e = 0; e < 4; ++e) acc[i][n][e] = (acc[i][n][e] + bb[i]) * (1.0f - gt[i]);
  for (int t = K2_T1; t < K2_T - 2; t += 2) {
    step2(fx, fy);
    step2(fy, fx);
  }
  step2(fx, fy);
  mm(fy);
#pragma unroll
  for (int i = 0; i < 4; ++i)
#pragma unroll
    for (int n = 0; n < 2; ++n) {
      const int s = tile_s * 128 + ws_ * 64 + i * 16 + (lane & 15);
      const int d = tile_d * 64 + wd * 32 + n * 16 + g * 4;
      *(u32x2*)(out + ((size_t)s * D_ + d) * 2) = u32x2{pack2(acc[i][n][0], acc[i][n][1]), pack2(acc[i][n][2], acc[i][n][3])};
    }
}

extern "C" void kernel_launch(void* const* d_in, const int* in_sizes, int n_in, void* d_out, int out_size, void* d_ws,
                              size_t ws_size, hipStream_t stream) {
  const char* x = (const char*)d_in[0];
  const char* wqkv = (const char*)d_in[1];
  const char* wo = (const char*)d_in[2];
  const char* w1 = (const char*)d_in[4];
  const char* b1 = (const char*)d_in[5];
  const char* w2 = (const char*)d_in[6];
  const char* b2 = (const char*)d_in[7];
  const char* gw = (const char*)d_in[8];
  char* ws = (char*)d_ws;
  (void)hipFuncSetAttribute((const void*)k1_gemms, hipFuncAttributeMaxDynamicSharedMemorySize, K1_LDS);
  (void)hipFuncSetAttribute((const void*)k2_out, hipFuncAttributeMaxDynamicSharedMemorySize, K2_LDS);
  k0_prep<<<256, 128, 0, stream>>>((const u32x4*)x, (const u32x4*)gw, ws);
  k1_gemms<<<256, 512, K1_LDS, stream>>>(wqkv, w1, b1, ws);
  k2_out<<<256, 512, K2_LDS, stream>>>(wo, w2, b2, ws, (char*)d_out);
}
```

```cpp
#include <hip/hip_runtime.h>
#include <hip/hip_bf16.h>

typedef __attribute__((ext_vector_type(2))) __bf16 bf16x2_t;
typedef __attribute__((ext_vector_type(8))) __bf16 bf16x8_t;
typedef __attribute__((ext_vector_type(4))) short s16x4;
typedef __attribute__((ext_vector_type(8))) short s16x8;
typedef __attribute__((ext_vector_type(4))) float f32x4;
typedef __attribute__((ext_vector_type(2))) float f32x2;
typedef __attribute__((ext_vector_type(4))) unsigned u32x4;
typedef __attribute__((ext_vector_type(2))) unsigned u32x2;
typedef __attribute__((address_space(3))) char* lds_ptr;

#define DI __device__ __forceinline__

constexpr int S_ = 2048, D_ = 1024, F_ = 3584;

constexpr size_t WS_XT = 0;
constexpr size_t WS_HT = WS_XT + (size_t)S_ * D_ * 2;
constexpr size_t WS_OT = WS_HT + (size_t)S_ * D_ * 2;
constexpr size_t WS_UT = WS_OT + (size_t)S_ * D_ * 2;
constexpr size_t WS_G = WS_UT + (size_t)D_ * F_ * 2;

struct Stamps {
  DI void at(int) {}
  DI void out(const char*, int) {}
};

DI unsigned pack2(float a, float b) {
  f32x2 f = {a, b};
  return __builtin_bit_cast(unsigned, __builtin_convertvector(f, bf16x2_t));
}
DI float bf_lo(unsigned u) { return __builtin_bit_cast(float, u << 16); }
DI float bf_hi(unsigned u) { return __builtin_bit_cast(float, u & 0xffff0000u); }

DI float dot2(unsigned a, unsigned b, float c) {
  return __builtin_amdgcn_fdot2_f32_bf16(__builtin_bit_cast(bf16x2_t, a), __builtin_bit_cast(bf16x2_t, b), c, false);
}

DI void glds16(unsigned lds_dst, unsigned voff, const void* sbase) {
  asm volatile("s_mov_b32 m0, %0\n\ts_nop 0\n\tglobal_load_lds_dwordx4 %1, %2" ::"s"(lds_dst), "v"(voff), "s"(sbase)
               : "memory");
}
template <int N>
DI void wait_vm() {
  asm volatile("s_waitcnt vmcnt(%0)" ::"i"(N) : "memory");
}
DI void bar_raw() { asm volatile("s_barrier" ::: "memory"); }
DI void bar_lgkm() { asm volatile("s_waitcnt lgkmcnt(0)\n\ts_barrier" ::: "memory"); }

DI bf16x8_t lds_b128(unsigned addr) {
  return *(const __attribute__((address_space(3))) bf16x8_t*)(size_t)addr;
}
DI bf16x8_t lds_tr(unsigned addr, unsigned off2) {
  s16x4 lo = __builtin_amdgcn_ds_read_tr16_b64_v4i16((__attribute__((address_space(3))) s16x4*)(size_t)addr);
  s16x4 hi = __builtin_amdgcn_ds_read_tr16_b64_v4i16((__attribute__((address_space(3))) s16x4*)(size_t)(addr + off2));
  s16x8 r = __builtin_shufflevector(lo, hi, 0, 1, 2, 3, 4, 5, 6, 7);
  return __builtin_bit_cast(bf16x8_t, r);
}
DI f32x4 mfma(bf16x8_t a, bf16x8_t b, f32x4 c) { return __builtin_amdgcn_mfma_f32_16x16x32_bf16(a, b, c, 0, 0, 0); }

template <int N, int KMAX, int X = 0>
DI void wait_vm_k(int k) {
  if (k >= KMAX) wait_vm<N * KMAX + X>();
  else if constexpr (KMAX > 0) wait_vm_k<N, KMAX - 1, X>(k);
}

template <int NP>
DI void warm(const char* base, unsigned piece0, unsigned npieces, unsigned lds_dummy) {
  const unsigned lane16 = (threadIdx.x & 63) * 16;
#pragma unroll
  for (int i = 0; i < NP; ++i) {
    const unsigned pc = min(piece0 + i, npieces - 1);
    glds16(lds_dummy, pc * 1024 + lane16, base);
  }
}

template <int CTRL>
DI float dpp_add(float v) {
  return v + __builtin_bit_cast(float, __builtin_amdgcn_update_dpp(0, __builtin_bit_cast(int, v), CTRL, 0xf, 0xf, true));
}
DI float rdlane(float v, int l) { return __builtin_bit_cast(float, __builtin_amdgcn_readlane(__builtin_bit_cast(int, v), l)); }
DI float wave_sum(float v) {
  v = dpp_add<0xB1>(v);
  v = dpp_add<0x4E>(v);
  v = dpp_add<0x141>(v);
  v = dpp_add<0x140>(v);
  return (rdlane(v, 0) + rdlane(v, 16)) + (rdlane(v, 32) + rdlane(v, 48));
}
DI void gload16(u32x4& d, unsigned voff, const void* sbase) {
  asm volatile("global_load_dwordx4 %0, %1, %2" : "=v"(d) : "v"(voff), "s"(sbase) : "memory");
}

constexpr int K0_PITCH = 2048 + 16;
__global__ __launch_bounds__(512) void k0_prep(const char* __restrict__ x, const char* __restrict__ gw, char* __restrict__ ws,
                                               const char* __restrict__ w1) {
  __shared__ __attribute__((aligned(16))) char lds[2 * 8 * K0_PITCH + 1024];
  const unsigned lx = (unsigned)(size_t)(lds_ptr)lds, lh = lx + 8 * K0_PITCH;
  const int t = threadIdx.x, lane = t & 63;
  const int wv = __builtin_amdgcn_readfirstlane(t >> 6);
  const int s0 = blockIdx.x * 8;
  u32x4 raw[2], gwv[2];
  gload16(raw[0], (s0 + wv) * 2048 + lane * 16, x);
  gload16(raw[1], (s0 + wv) * 2048 + 1024 + lane * 16, x);
  gload16(gwv[0], lane * 16, gw);
  gload16(gwv[1], 1024 + lane * 16, gw);
  warm<7>(w1, (blockIdx.x * 8 + wv) * 7, S_ * F_ * 2 / 1024, lh + 8 * K0_PITCH);
  asm volatile("s_waitcnt vmcnt(7)" : "+v"(raw[0]), "+v"(raw[1]), "+v"(gwv[0]), "+v"(gwv[1]));
  typedef __attribute__((address_space(3))) u32x4* l128;
  *(l128)(size_t)(lx + wv * K0_PITCH + lane * 16) = raw[0];
  *(l128)(size_t)(lx + wv * K0_PITCH + 1024 + lane * 16) = raw[1];
  float v[16], sm = 0.f, gd = 0.f;
#pragma unroll
  for (int i = 0; i < 8; ++i) {
    const unsigned r = raw[i >> 2][i & 3], g = gwv[i >> 2][i & 3];
    v[2 * i] = bf_lo(r); v[2 * i + 1] = bf_hi(r);
    sm += v[2 * i] + v[2 * i + 1];
    gd += v[2 * i] * bf_lo(g) + v[2 * i + 1] * bf_hi(g);
  }
  const float mean = wave_sum(sm) * (1.0f / D_);
  gd = wave_sum(gd);
  float vr = 0.f;
#pragma unroll
  for (int i = 0; i < 16; ++i) { v[i] -= mean; vr += v[i] * v[i]; }
  const float rs = rsqrtf(wave_sum(vr) * (1.0f / D_) + 1e-5f);
#pragma unroll
  for (int h = 0; h < 2; ++h) {
    u32x4 o;
#pragma unroll
    for (int i = 0; i < 4; ++i) o[i] = pack2(v[8 * h + 2 * i] * rs, v[8 * h + 2 * i + 1] * rs);
    *(l128)(size_t)(lh + wv * K0_PITCH + h * 1024 + lane * 16) = o;
  }
  if (lane == 0) ((float*)(ws + WS_G))[s0 + wv] = 1.0f / (1.0f + __expf(-gd));
  bar_lgkm();
#pragma unroll
  for (int h = 0; h < 2; ++h) {
    const int ch = h * 64 + (t >> 3), r = t & 7;
    const u32x4 o = *(l128)(size_t)(lx + r * K0_PITCH + ch * 16);
    *(u32x4*)(ws + WS_XT + ((size_t)((ch >> 2) * 128 + (s0 >> 4)) * 64 + (ch & 3) * 16 + (s0 & 15) + r) * 16) = o;
    const int d0 = wv * 128 + h * 64;
    const bf16x8_t c = lds_tr(lh + ((lane & 15) >> 2) * K0_PITCH + (d0 + (lane >> 4) * 16 + (lane & 3) * 4) * 2, 4 * K0_PITCH);
    const int d = d0 + lane;
    *(bf16x8_t*)(ws + WS_HT + ((size_t)((s0 >> 5) * 64 + (d >> 4)) * 64 + ((s0 & 31) >> 3) * 16 + (d & 15)) * 16) = c;
  }
  wait_vm<0>();
}

DI int sw_std(int r) { return ((r >> 1) & 1) | (((r >> 3) & 1) << 1); }
DI int sw_prm(int r) { return (r >> 1) & 3; }

constexpr int G1_STAGE = 29 * 1024, G1_NS = 5, G1_T = 32;
constexpr int G3_STAGE = 22 * 1024, G3_NS = 6, G3_T = 64;
constexpr int K1_DUMMY = G1_STAGE * G1_NS;
constexpr int K1_LDS = K1_DUMMY + 1024;
static_assert(G3_STAGE * G3_NS <= K1_DUMMY, "");
constexpr int G1_PF = 8, G3_PF = 16;

struct G1Frags { bf16x8_t a[4], ah, b[6], bh; };

DI void g1_read(G1Frags& f, unsigned so, unsigned a_base, unsigned ah_base, unsigned b0, unsigned b1, unsigned bh) {
#pragma unroll
  for (int p = 0; p < 3; ++p) {
    f.b[2 * p] = lds_tr(b0 + so + p * 4096, 512);
    f.b[2 * p + 1] = lds_tr(b1 + so + p * 4096, 512);
  }
#pragma unroll
  for (int m = 0; m < 4; ++m) f.a[m] = lds_b128(a_base + so + m * 1024);
  f.bh = lds_tr(bh + so, 512);
  f.ah = lds_b128(ah_base + so);
}
DI void g1_mma(f32x4 (&acc)[4][6], f32x4& acch, const G1Frags& f) {
#pragma unroll
  for (int m = 0; m < 4; ++m)
#pragma unroll
    for (int n = 0; n < 6; ++n) acc[m][n] = mfma(f.b[n], f.a[m], acc[m][n]);
  acch = mfma(f.bh, f.ah, acch);
}
template <int NMFMA, int NDS>
DI void interleave() {
#pragma unroll
  for (int i = 0; i < NMFMA; ++i) {
    __builtin_amdgcn_sched_group_barrier(0x008, 1, 0);
    if (i < NDS) __builtin_amdgcn_sched_group_barrier(0x100, 1, 0);
    __builtin_amdgcn_sched_group_barrier(0x002, 1, 0);
  }
}

template <int NMFMA, int NDS>
DI void interleave2() {
#pragma unroll
  for (int i = 0; i < NMFMA; ++i) {
    __builtin_amdgcn_sched_group_barrier(0x008, 1, 0);
    if (2 * i < NDS) __builtin_amdgcn_sched_group_barrier(0x100, 2, 0);
    __builtin_amdgcn_sched_group_barrier(0x002, 1, 0);
  }
}

DI void gemm1_attn(int tile_m, int tile_n, const char* __restrict__ wqkv, char* __restrict__ ws, const char* pfb, unsigned pf0, unsigned pfn) {
  const int tid = threadIdx.x, lane = tid & 63;
  const int w = __builtin_amdgcn_readfirstlane(tid >> 6);
  const int wm = w >> 1, wn = w & 1;
  Stamps stp; stp.at(0);
  const char* srcA = ws + WS_XT;
  const char* srcB = wqkv + (size_t)tile_n * 128;
  const unsigned voffA = (unsigned)(tile_m * 16 + 2 * w) * 1024 + lane * 16;
  const unsigned voffH = (unsigned)(tile_m > 0 ? tile_m * 16 - 1 : 0) * 1024 + lane * 16;
  auto voffB = [&](int n) {
    const int r = (n & 3) * 8 + (lane >> 3), c = (lane & 7) ^ (sw_std(r) << 1);
    return (unsigned)(r * (3 * D_ * 2) + (n >> 2) * (D_ * 2) + c * 16);
  };
  const unsigned voffB0 = voffB(w);
  const unsigned voffX = (w < 4) ? voffB(8 + w) : voffH;
  auto issue = [&](int st, unsigned slot) {
    const char* a = srcA + (size_t)st * (128 * 1024);
    const char* b = srcB + (size_t)st * (32 * 3 * D_ * 2);
    glds16(slot + 2 * w * 1024, voffA, a);
    glds16(slot + 2 * w * 1024 + 1024, voffA + 1024, a);
    glds16(slot + 17 * 1024 + w * 1024, voffB0, b);
    if (w < 4) glds16(slot + 17 * 1024 + (8 + w) * 1024, voffX, b);
    else if (w == 4) glds16(slot + 16 * 1024, voffX, a);
  };
  auto waitk = [&](int k) {
    if (w <= 4) wait_vm_k<4, G1_NS - 1>(k);
    else wait_vm_k<3, G1_NS - 1>(k);
  };
  auto waitx = [&](int k) {
    if (w <= 4) wait_vm_k<4, G1_NS - 1, G1_PF>(k);
    else wait_vm_k<3, G1_NS - 1, G1_PF>(k);
  };
  const int hd = tid >> 8, row = tid & 255, s = tile_m * 256 + row;
  float gate = ((const float*)(ws + WS_G))[s];
  asm volatile("" : "+v"(gate));
  const unsigned a_base = wm * 4096 + lane * 16;
  const unsigned ah_base = 16 * 1024 + lane * 16;
  const int trr = (lane >> 4) * 8 + ((lane & 15) >> 2), trp = lane & 3;
  auto tr_addr = [&](int jb) { return (unsigned)(17 * 1024 + trr * 128 + ((jb ^ sw_std(trr)) * 32) + trp * 8); };
  const unsigned b0 = tr_addr(2 * wn), b1 = tr_addr(2 * wn + 1);
  const unsigned bh = tr_addr(2 * wn + (wm & 1)) + (1 + (wm >> 1)) * 4096;

  f32x4 acc[4][6], acch = {0.f, 0.f, 0.f, 0.f};
#pragma unroll
  for (int m = 0; m < 4; ++m)
#pragma unroll
    for (int n = 0; n < 6; ++n) acc[m][n] = f32x4{0.f, 0.f, 0.f, 0.f};

#pragma unroll
  for (int s = 0; s < G1_NS; ++s) issue(s, s * G1_STAGE);
  waitk(G1_NS - 1);
  bar_raw();
  stp.at(1);
  G1Frags fx, fy;
  g1_read(fx, 0, a_base, ah_base, b0, b1, bh);
  unsigned cur = 0;
  auto step = [&](int t, G1Frags& fc, G1Frags& fn) {
    if (t + G1_NS <= G1_T) waitk(G1_NS - 2);
    else waitx(G1_T - 2 - t);
    bar_lgkm();
    if (t + G1_NS < G1_T) issue(t + G1_NS, cur);
    else if (t + G1_NS == G1_T) warm<G1_PF>(pfb, pf0 + w * G1_PF, pfn, K1_DUMMY);
    cur += G1_STAGE;
    if (cur == G1_NS * G1_STAGE) cur = 0;
    __builtin_amdgcn_sched_barrier(0);
    g1_read(fn, cur, a_base, ah_base, b0, b1, bh);
    g1_mma(acc, acch, fc);
    interleave<25, 19>();
    __builtin_amdgcn_sched_barrier(0);
  };
  for (int t = 0; t < G1_T - 2; t += 2) {
    step(t, fx, fy);
    step(t + 1, fy, fx);
  }
  step(G1_T - 2, fx, fy);
  g1_mma(acc, acch, fy);
  bar_lgkm();
  stp.at(2);

  constexpr int PL = 272 * 16;
  {
    const unsigned wbase = wn * (12 * PL) + (lane >> 5) * PL + ((lane >> 4) & 1) * 8 + (lane & 15) * 16;
#pragma unroll
    for (int m = 0; m < 4; ++m)
#pragma unroll
      for (int n = 0; n < 6; ++n) {
        u32x2 o = {pack2(acc[m][n][0], acc[m][n][1]), pack2(acc[m][n][2], acc[m][n][3])};
        *(__attribute__((address_space(3))) u32x2*)(size_t)(wbase + n * 2 * PL + (16 + wm * 64 + m * 16) * 16) = o;
      }
    u32x2 o = {pack2(acch[0], acch[1]), pack2(acch[2], acch[3])};
    *(__attribute__((address_space(3))) u32x2*)(size_t)(wbase + (2 + wm) * 2 * PL) = o;
  }
  bar_lgkm();
  {
    const unsigned rbase = hd * (12 * PL) + (16 + row) * 16;
    auto ld = [&](int mat, int c, int back) {
      return *(const __attribute__((address_space(3))) u32x4*)(size_t)(rbase + (mat * 4 + c) * PL - back * 32);
    };
    u32x4 q[4];
#pragma unroll
    for (int c = 0; c < 4; ++c) q[c] = ld(0, c, 0);
    float lg[8];
#pragma unroll
    for (int wd = 0; wd < 8; ++wd) {
      float a = 0.f;
#pragma unroll
      for (int c = 0; c < 4; ++c) {
        u32x4 kk = ld(1, c, wd);
#pragma unroll
        for (int i = 0; i < 4; ++i)
          a = dot2(q[c][i], kk[i], a);
      }
      lg[wd] = (s - 2 * wd >= 0) ? a * 0.17677669529663687f : -1e9f;
    }
    float mx = lg[0];
#pragma unroll
    for (int wd = 1; wd < 8; ++wd) mx = fmaxf(mx, lg[wd]);
    float sum = 0.f;
#pragma unroll
    for (int wd = 0; wd < 8; ++wd) { lg[wd] = __expf(lg[wd] - mx); sum += lg[wd]; }
    const float sc = gate / sum;
    float o[32];
#pragma unroll
    for (int i = 0; i < 32; ++i) o[i] = 0.f;
#pragma unroll
    for (int wd = 0; wd < 8; ++wd) {
      const bool ok = (s - 2 * wd >= 0);
      const float p = lg[wd] * sc;
#pragma unroll
      for (int c = 0; c < 4; ++c) {
        u32x4 vv = ld(2, c, ok ? wd : 0);
#pragma unroll
        for (int i = 0; i < 4; ++i) {
          o[c * 8 + 2 * i] += p * bf_lo(vv[i]);
          o[c * 8 + 2 * i + 1] += p * bf_hi(vv[i]);
        }
      }
    }
    u32x4* dst = (u32x4*)(ws + WS_OT) + ((size_t)((tile_n * 2 + hd) * 128 + (s >> 4)) * 64 + (s & 15));
#pragma unroll
    for (int qq = 0; qq < 4; ++qq) {
      u32x4 r = {pack2(o[4 * qq], o[4 * qq + 1]), pack2(o[4 * qq + 2], o[4 * qq + 3]),
                 pack2(o[16 + 4 * qq], o[16 + 4 * qq + 1]), pack2(o[16 + 4 * qq + 2], o[16 + 4 * qq + 3])};
      dst[qq * 16] = r;
    }
  }
  wait_vm<0>();
  stp.at(3); stp.out(ws, 0);
}

struct G3Frags { bf16x8_t h[4], w[7]; };

DI float gelu_tanh(float u) {
  const float c0 = -2.0f * 0.7978845608028654f * 1.4426950408889634f;
  const float c1 = c0 * 0.044715f;
  const float e = __builtin_amdgcn_exp2f(u * __builtin_fmaf(u * u, c1, c0));
  return u * __builtin_amdgcn_rcpf(1.0f + e);
}

DI void gemm3_gelu(int tile_d, int tile_f, const char* __restrict__ w1, const char* __restrict__ b1, char* __restrict__ ws, const char* pfb, unsigned pf0, unsigned pfn) {
  const int tid = threadIdx.x, lane = tid & 63;
  const int w = __builtin_amdgcn_readfirstlane(tid >> 6);
  const int odd = tile_f & 1;
  Stamps stp; stp.at(0);
  if (w >= 4) {
    const int lw = w - 4;
    const char* srcA = ws + WS_HT;
    const char* srcB = w1 + (size_t)tile_f * 448;
    const unsigned voffA = (unsigned)(tile_d * 8 + 2 * lw) * 1024 + lane * 16;
    const int r = lw * 8 + (lane >> 3), c = (lane & 7) ^ (sw_std(r) << 1);
    const unsigned voffB = (unsigned)(r * (F_ * 2) + odd * 64 + c * 16);
    const int hg = (lw & 1) * 2 + ((lane >> 4) & 1), hr = hg * 8 + (lane >> 5) * 4 + ((lane >> 2) & 3);
    const int hc = (lane & 3) ^ ((hg & 1) << 1);
    const unsigned voffH = (unsigned)(hr * (F_ * 2) + (odd ? 0 : 384) + hc * 16);
    auto issue = [&](int st, unsigned slot) {
      const char* a = srcA + (size_t)st * (64 * 1024);
      const char* b = srcB + (size_t)st * (32 * F_ * 2);
      glds16(slot + 2 * lw * 1024, voffA, a);
      glds16(slot + 2 * lw * 1024 + 1024, voffA + 1024, a);
      glds16(slot + 8192 + lw * 1024, voffB, b);
      glds16(slot + 8192 + 4096 + lw * 1024, voffB + 128, b);
      glds16(slot + 8192 + 8192 + lw * 1024, voffB + 256, b);
      if (lw < 2) glds16(slot + 8192 + 12288 + lw * 1024, voffH, b);
    };
    auto waitk = [&](int k) {
      if (lw < 2) wait_vm_k<6, G3_NS - 1>(k);
      else wait_vm_k<5, G3_NS - 1>(k);
    };
    auto waitx = [&](int k) {
      if (lw < 2) wait_vm_k<6, G3_NS - 1, G3_PF>(k);
      else wait_vm_k<5, G3_NS - 1, G3_PF>(k);
    };
#pragma unroll
    for (int s = 0; s < G3_NS; ++s) issue(s, s * G3_STAGE);
    waitk(G3_NS - 1);
    bar_raw();
    unsigned cur = 0;
    for (int t = 0; t < G3_T - 1; ++t) {
      if (t + G3_NS <= G3_T) waitk(G3_NS - 2);
      else waitx(G3_T - 2 - t);
      bar_raw();
      if (t + G3_NS < G3_T) issue(t + G3_NS, cur);
      else if (t + G3_NS == G3_T) warm<G3_PF>(pfb, pf0 + lw * G3_PF, pfn, K1_DUMMY);
      cur += G3_STAGE;
      if (cur == G3_NS * G3_STAGE) cur = 0;
    }
    wait_vm<0>();
    return;
  }
  const int wd = w >> 1, wf = w & 1;
  const unsigned h_base = wd * 4096 + lane * 16;
  const int g = lane >> 4, qq = (lane & 15) >> 2, trp = lane & 3, trr = g * 8 + qq;
  unsigned wb[7];
  int fcol[7];
#pragma unroll
  for (int i = 0; i < 7; ++i) {
    const int lt = wf * 7 + i;
    const unsigned pa = 8192 + (lt >> 2) * 4096 + trr * 128 + (((lt & 3) ^ sw_std(trr)) * 32) + trp * 8;
    const unsigned ha = 8192 + 12288 + (g >> 1) * 1024 + (g & 1) * 256 + qq * 64 + (((lt & 1) ^ (g & 1)) * 32) + trp * 8;
    wb[i] = lt < 12 ? pa : ha;
    fcol[i] = tile_f * 224 + (odd ? (lt < 12 ? 32 + 16 * lt : 16 * (lt - 12)) : 16 * lt);
  }
  u32x2 bias[7];
#pragma unroll
  for (int i = 0; i < 7; ++i) bias[i] = *(const u32x2*)(b1 + (size_t)(fcol[i] + 4 * g) * 2);

  f32x4 acc[7][4];
#pragma unroll
  for (int i = 0; i < 7; ++i)
#pragma unroll
    for (int m = 0; m < 4; ++m) acc[i][m] = f32x4{0.f, 0.f, 0.f, 0.f};
  auto rd = [&](G3Frags& f, unsigned so) {
#pragma unroll
    for (int m = 0; m < 4; ++m) f.h[m] = lds_b128(h_base + so + m * 1024);
#pragma unroll
    for (int i = 0; i < 7; ++i) f.w[i] = lds_tr(wb[i] + so, 512);
  };
  auto mm = [&](const G3Frags& f) {
#pragma unroll
    for (int i = 0; i < 7; ++i)
#pragma unroll
      for (int m = 0; m < 4; ++m) acc[i][m] = mfma(f.w[i], f.h[m], acc[i][m]);
  };
#pragma unroll
  for (int i = 0; i < 7; ++i) asm volatile("" : "+v"(bias[i]));
  G3Frags fx, fy;
  bar_raw();
  stp.at(1);
  rd(fx, 0);
  unsigned cur = 0;
  auto step = [&](G3Frags& fc, G3Frags& fn) {
    bar_lgkm();
    cur += G3_STAGE;
    if (cur == G3_NS * G3_STAGE) cur = 0;
    __builtin_amdgcn_sched_barrier(0);
    rd(fn, cur);
    mm(fc);
    interleave<28, 18>();
    __builtin_amdgcn_sched_barrier(0);
  };
  for (int t = 0; t < G3_T - 2; t += 2) {
    step(fx, fy);
    step(fy, fx);
  }
  step(fx, fy);
  mm(fy);
  stp.at(2);
  char* ut = ws + WS_UT;
#pragma unroll
  for (int m = 0; m < 4; ++m) {
    u32x2 pk[7];
#pragma unroll
    for (int i = 0; i < 7; ++i) {
      const float v0 = gelu_tanh(acc[i][m][0] + bf_lo(bias[i][0])), v1 = gelu_tanh(acc[i][m][1] + bf_hi(bias[i][0]));
      const float v2 = gelu_tanh(acc[i][m][2] + bf_lo(bias[i][1])), v3 = gelu_tanh(acc[i][m][3] + bf_hi(bias[i][1]));
      pk[i] = u32x2{pack2(v0, v1), pack2(v2, v3)};
    }
    const size_t rbo = (size_t)(tile_d * 8 + wd * 4 + m) * 1024 + lane * 16;
    if (wf == 0) {
#pragma unroll
      for (int p = 0; p < 3; ++p)
        *(u32x4*)(ut + (size_t)(fcol[2 * p] >> 5) * 65536 + rbo) = u32x4{pk[2 * p][0], pk[2 * p][1], pk[2 * p + 1][0], pk[2 * p + 1][1]};
      *(u32x2*)(ut + (size_t)(fcol[6] >> 5) * 65536 + rbo) = pk[6];
    } else {
      *(u32x2*)(ut + (size_t)(fcol[0] >> 5) * 65536 + rbo + 8) = pk[0];
#pragma unroll
      for (int p = 0; p < 3; ++p)
        *(u32x4*)(ut + (size_t)(fcol[2 * p + 1] >> 5) * 65536 + rbo) =
            u32x4{pk[2 * p + 1][0], pk[2 * p + 1][1], pk[2 * p + 2][0], pk[2 * p + 2][1]};
    }
  }
  stp.at(3); stp.out(ws, 0);
}

__global__ __launch_bounds__(512, 2) void k1_gemms(const char* __restrict__ wqkv, const char* __restrict__ w1,
                                                   const char* __restrict__ b1, char* __restrict__ ws,
                                                   const char* __restrict__ w2, const char* __restrict__ wo) {
  const int b = blockIdx.x, xcd = b & 7, j = b >> 3;
  const char* pfb = b < 224 ? w2 : wo;
  const unsigned pf0 = (b < 224 ? b : b - 224) * 64, pfn = b < 224 ? F_ * S_ * 2 / 1024 : D_ * D_ * 2 / 1024;
  if (xcd < 4) gemm1_attn((xcd & 1) * 4 + (j & 3), (xcd >> 1) * 8 + (j >> 2), wqkv, ws, pfb, pf0, pfn);
  else gemm3_gelu(j & 7, (xcd - 4) * 4 + (j >> 3), w1, b1, ws, pfb, pf0, pfn);
}

constexpr int K2_STAGE = 24 * 1024, K2_NS = 6, K2_T1 = 56, K2_T = 72;
constexpr int K2_LDS = K2_STAGE * K2_NS;

struct K2Frags { bf16x8_t s[2][4], d[2][2]; };

__global__ __launch_bounds__(512, 2) void k2_out(const char* __restrict__ wo, const char* __restrict__ w2,
                                                 const char* __restrict__ b2, const char* __restrict__ ws,
                                                 char* __restrict__ out) {
  const int b = blockIdx.x, xcd = b & 7, j = b >> 3;
  const int tile_s = (xcd >> 1) * 4 + (j & 3), tile_d = (xcd & 1) * 8 + (j >> 2);
  const int tid = threadIdx.x, lane = tid & 63;
  const int w = __builtin_amdgcn_readfirstlane(tid >> 6);
  Stamps stp; stp.at(0);
  if (w >= 4) {
    const int lw = w - 4;
    const int r = lw * 8 + (lane >> 3), c = (lane & 7) ^ (sw_prm(r) << 1);
    const unsigned voffW2 = (unsigned)(r * (S_ * 2) + tile_s * 256 + c * 16);
    const unsigned voffU = (unsigned)(tile_d * 4 + lw) * 1024 + lane * 16;
    const unsigned voffO = (unsigned)(tile_s * 8 + 2 * lw) * 1024 + lane * 16;
    const unsigned voffWo = (unsigned)(r * (D_ * 2) + tile_d * 128 + c * 16);
    auto issue = [&](int st, unsigned slot) {
      if (st < K2_T1) {
        const char* a = w2 + (size_t)st * (64 * S_ * 2);
        const char* u = ws + WS_UT + (size_t)st * (2 * 65536);
#pragma unroll
        for (int kk = 0; kk < 2; ++kk) {
          glds16(slot + kk * 8192 + lw * 1024, voffW2 + kk * (32 * S_ * 2), a);
          glds16(slot + kk * 8192 + 4096 + lw * 1024, voffW2 + kk * (32 * S_ * 2) + 128, a);
          glds16(slot + 16384 + kk * 4096 + lw * 1024, voffU + kk * 65536, u);
        }
      } else {
        const char* o = ws + WS_OT + (size_t)(st - K2_T1) * (2 * 131072);
        const char* a = wo + (size_t)(st - K2_T1) * (64 * D_ * 2);
#pragma unroll
        for (int kk = 0; kk < 2; ++kk) {
          glds16(slot + kk * 8192 + 2 * lw * 1024, voffO + kk * 131072, o);
          glds16(slot + kk * 8192 + 2 * lw * 1024 + 1024, voffO + kk * 131072 + 1024, o);
          glds16(slot + 16384 + kk * 4096 + lw * 1024, voffWo + kk * (32 * D_ * 2), a);
        }
      }
    };
#pragma unroll
    for (int s = 0; s < K2_NS; ++s) issue(s, s * K2_STAGE);
    wait_vm<6 * (K2_NS - 1)>();
    bar_raw();
    unsigned cur = 0;
    for (int t = 0; t < K2_T - 1; ++t) {
      wait_vm_k<6, K2_NS - 2>(min(K2_NS - 2, K2_T - 2 - t));
      bar_raw();
      if (t + K2_NS < K2_T) issue(t + K2_NS, cur);
      cur += K2_STAGE;
      if (cur == K2_NS * K2_STAGE) cur = 0;
    }
    return;
  }
  const int ws_ = w >> 1, wd = w & 1;
  const int g = lane >> 4, trr = g * 4 + ((lane & 15) >> 2), trp = lane & 3;
  unsigned ta[4];
#pragma unroll
  for (int i = 0; i < 4; ++i) ta[i] = trr * 128 + ((i ^ sw_prm(trr)) * 32) + trp * 8;
  const unsigned tw0 = trr * 128 + (((2 * wd) ^ sw_prm(trr)) * 32) + trp * 8 + 16384;
  const unsigned tw1 = trr * 128 + (((2 * wd + 1) ^ sw_prm(trr)) * 32) + trp * 8 + 16384;
  const unsigned lin = lane * 16;
  float gt[4], bb[4];
#pragma unroll
  for (int i = 0; i < 4; ++i) {
    const int s = tile_s * 128 + ws_ * 64 + i * 16 + (lane & 15);
    gt[i] = ((const float*)(ws + WS_G))[s];
    bb[i] = bf_lo(((const unsigned short*)b2)[s]);
  }
  f32x4 acc[4][2];
#pragma unroll
  for (int i = 0; i < 4; ++i)
#pragma unroll
    for (int n = 0; n < 2; ++n) acc[i][n] = f32x4{0.f, 0.f, 0.f, 0.f};
  auto rd1 = [&](K2Frags& f, unsigned so) {
#pragma unroll
    for (int kk = 0; kk < 2; ++kk) {
#pragma unroll
      for (int n = 0; n < 2; ++n) f.d[kk][n] = lds_b128(lin + so + 16384 + kk * 4096 + (wd * 2 + n) * 1024);
#pragma unroll
      for (int i = 0; i < 4; ++i) f.s[kk][i] = lds_tr(ta[i] + so + kk * 8192 + ws_ * 4096, 2048);
    }
  };
  auto rd2 = [&](K2Frags& f, unsigned so) {
#pragma unroll
    for (int kk = 0; kk < 2; ++kk) {
      f.d[kk][0] = lds_tr(tw0 + so + kk * 4096, 2048);
      f.d[kk][1] = lds_tr(tw1 + so + kk * 4096, 2048);
#pragma unroll
      for (int i = 0; i < 4; ++i) f.s[kk][i] = lds_b128(lin + so + kk * 8192 + (ws_ * 4 + i) * 1024);
    }
  };
  auto mm = [&](const K2Frags& f) {
#pragma unroll
    for (int kk = 0; kk < 2; ++kk)
#pragma unroll
      for (int i = 0; i < 4; ++i)
#pragma unroll
        for (int n = 0; n < 2; ++n) acc[i][n] = mfma(f.d[kk][n], f.s[kk][i], acc[i][n]);
  };
#pragma unroll
  for (int i = 0; i < 4; ++i) asm volatile("" : "+v"(gt[i]), "+v"(bb[i]));
  K2Frags fx, fy;
  bar_raw();
  stp.at(1);
  rd1(fx, 0);
  unsigned cur = 0;
  auto adv = [&]() {
    bar_lgkm();
    cur += K2_STAGE;
    if (cur == K2_NS * K2_STAGE) cur = 0;
    __builtin_amdgcn_sched_barrier(0);
  };
  auto step1 = [&](K2Frags& fc, K2Frags& fn) { adv(); rd1(fn, cur); mm(fc); interleave2<16, 20>(); __builtin_amdgcn_sched_barrier(0); };
  auto step2 = [&](K2Frags& fc, K2Frags& fn) { adv(); rd2(fn, cur); mm(fc); interleave2<16, 16>(); __builtin_amdgcn_sched_barrier(0); };
  for (int t = 0; t < K2_T1 - 2; t += 2) {
    step1(fx, fy);
    step1(fy, fx);
  }
  step1(fx, fy);
  step2(fy, fx);
#pragma unroll
  for (int i = 0; i < 4; ++i)
#pragma unroll
    for (int n = 0; n < 2; ++n)
#pragma unroll
      for (int e = 0; e < 4; ++e) acc[i][n][e] = (acc[i][n][e] + bb[i]) * (1.0f - gt[i]);
  for (int t = K2_T1; t < K2_T - 2; t += 2) {
    step2(fx, fy);
    step2(fy, fx);
  }
  step2(fx, fy);
  mm(fy);
  stp.at(2);
#pragma unroll
  for (int i = 0; i < 4; ++i)
#pragma unroll
    for (int n = 0; n < 2; ++n) {
      const int s = tile_s * 128 + ws_ * 64 + i * 16 + (lane & 15);
      const int d = tile_d * 64 + wd * 32 + n * 16 + g * 4;
      *(u32x2*)(out + ((size_t)s * D_ + d) * 2) = u32x2{pack2(acc[i][n][0], acc[i][n][1]), pack2(acc[i][n][2], acc[i][n][3])};
    }
  stp.at(3); stp.out(ws, 1);
}

extern "C" void kernel_launch(void* const* d_in, const int* in_sizes, int n_in, void* d_out, int out_size, void* d_ws,
                              size_t ws_size, hipStream_t stream) {
  const char* x = (const char*)d_in[0];
  const char* wqkv = (const char*)d_in[1];
  const char* wo = (const char*)d_in[2];
  const char* w1 = (const char*)d_in[4];
  const char* b1 = (const char*)d_in[5];
  const char* w2 = (const char*)d_in[6];
  const char* b2 = (const char*)d_in[7];
  const char* gw = (const char*)d_in[8];
  char* ws = (char*)d_ws;
  (void)hipFuncSetAttribute((const void*)k1_gemms, hipFuncAttributeMaxDynamicSharedMemorySize, K1_LDS);
  (void)hipFuncSetAttribute((const void*)k2_out, hipFuncAttributeMaxDynamicSharedMemorySize, K2_LDS);
  k0_prep<<<256, 512, 0, stream>>>(x, gw, ws, w1);
  k1_gemms<<<256, 512, K1_LDS, stream>>>(wqkv, w1, b1, ws, w2, wo);
  k2_out<<<256, 512, K2_LDS, stream>>>(wo, w2, b2, ws, (char*)d_out);
}
```

```cpp
#include <hip/hip_runtime.h>
#include <hip/hip_bf16.h>

typedef __attribute__((ext_vector_type(2))) __bf16 bf16x2_t;
typedef __attribute__((ext_vector_type(8))) __bf16 bf16x8_t;
typedef __attribute__((ext_vector_type(4))) short s16x4;
typedef __attribute__((ext_vector_type(8))) short s16x8;
typedef __attribute__((ext_vector_type(4))) float f32x4;
typedef __attribute__((ext_vector_type(2))) float f32x2;
typedef __attribute__((ext_vector_type(4))) unsigned u32x4;
typedef __attribute__((ext_vector_type(2))) unsigned u32x2;
typedef __attribute__((address_space(3))) char* lds_ptr;

#define DI __device__ __forceinline__

constexpr int S_ = 2048, D_ = 1024, F_ = 3584;

constexpr size_t WS_XT = 0;
constexpr size_t WS_HT = WS_XT + (size_t)S_ * D_ * 2;
constexpr size_t WS_OT = WS_HT + (size_t)S_ * D_ * 2;
constexpr size_t WS_UT = WS_OT + (size_t)S_ * D_ * 2;
constexpr size_t WS_G = WS_UT + (size_t)D_ * F_ * 2;

struct Stamps {
  DI void at(int) {}
  DI void out(const char*, int) {}
};

DI unsigned pack2(float a, float b) {
  f32x2 f = {a, b};
  return __builtin_bit_cast(unsigned, __builtin_convertvector(f, bf16x2_t));
}
DI float bf_lo(unsigned u) { return __builtin_bit_cast(float, u << 16); }
DI float bf_hi(unsigned u) { return __builtin_bit_cast(float, u & 0xffff0000u); }

DI float dot2(unsigned a, unsigned b, float c) {
  return __builtin_amdgcn_fdot2_f32_bf16(__builtin_bit_cast(bf16x2_t, a), __builtin_bit_cast(bf16x2_t, b), c, false);
}

DI void glds16(unsigned lds_dst, unsigned voff, const void* sbase) {
  asm volatile("s_mov_b32 m0, %0\n\ts_nop 0\n\tglobal_load_lds_dwordx4 %1, %2" ::"s"(lds_dst), "v"(voff), "s"(sbase)
               : "memory");
}
template <int N>
DI void wait_vm() {
  asm volatile("s_waitcnt vmcnt(%0)" ::"i"(N) : "memory");
}
DI void bar_raw() { asm volatile("s_barrier" ::: "memory"); }
DI void bar_lgkm() {
  __builtin_amdgcn_s_waitcnt(0xC07F);
  asm volatile("s_barrier" ::: "memory");
}

DI bf16x8_t lds_b128(unsigned addr) {
  return *(const __attribute__((address_space(3))) bf16x8_t*)(size_t)addr;
}
DI bf16x8_t lds_tr(unsigned addr, unsigned off2) {
  s16x4 lo = __builtin_amdgcn_ds_read_tr16_b64_v4i16((__attribute__((address_space(3))) s16x4*)(size_t)addr);
  s16x4 hi = __builtin_amdgcn_ds_read_tr16_b64_v4i16((__attribute__((address_space(3))) s16x4*)(size_t)(addr + off2));
  s16x8 r = __builtin_shufflevector(lo, hi, 0, 1, 2, 3, 4, 5, 6, 7);
  return __builtin_bit_cast(bf16x8_t, r);
}
DI f32x4 mfma(bf16x8_t a, bf16x8_t b, f32x4 c) { return __builtin_amdgcn_mfma_f32_16x16x32_bf16(a, b, c, 0, 0, 0); }

template <int V> struct IC { static constexpr int v = V; };
DI unsigned pin(unsigned v) { asm volatile("" : "+v"(v)); return v; }

template <int N, int KMAX, int X = 0>
DI void wait_vm_k(int k) {
  if (k >= KMAX) wait_vm<N * KMAX + X>();
  else if constexpr (KMAX > 0) wait_vm_k<N, KMAX - 1, X>(k);
}

template <int NP>
DI void warm(const char* base, unsigned piece0, unsigned npieces, unsigned lds_dummy) {
  const unsigned lane16 = (threadIdx.x & 63) * 16;
#pragma unroll
  for (int i = 0; i < NP; ++i) {
    const unsigned pc = min(piece0 + i, npieces - 1);
    glds16(lds_dummy, pc * 1024 + lane16, base);
  }
}

template <int CTRL>
DI float dpp_add(float v) {
  return v + __builtin_bit_cast(float, __builtin_amdgcn_update_dpp(0, __builtin_bit_cast(int, v), CTRL, 0xf, 0xf, true));
}
DI float rdlane(float v, int l) { return __builtin_bit_cast(float, __builtin_amdgcn_readlane(__builtin_bit_cast(int, v), l)); }
DI float wave_sum(float v) {
  v = dpp_add<0xB1>(v);
  v = dpp_add<0x4E>(v);
  v = dpp_add<0x141>(v);
  v = dpp_add<0x140>(v);
  return (rdlane(v, 0) + rdlane(v, 16)) + (rdlane(v, 32) + rdlane(v, 48));
}
DI void gload16(u32x4& d, unsigned voff, const void* sbase) {
  asm volatile("global_load_dwordx4 %0, %1, %2" : "=v"(d) : "v"(voff), "s"(sbase) : "memory");
}

constexpr int K0_PITCH = 2048 + 16;
__global__ __launch_bounds__(512) void k0_prep(const char* __restrict__ x, const char* __restrict__ gw, char* __restrict__ ws,
                                               const char* __restrict__ w1) {
  __shared__ __attribute__((aligned(16))) char lds[2 * 8 * K0_PITCH + 1024];
  const unsigned lx = (unsigned)(size_t)(lds_ptr)lds, lh = lx + 8 * K0_PITCH;
  const int t = threadIdx.x, lane = t & 63;
  const int wv = __builtin_amdgcn_readfirstlane(t >> 6);
  const int s0 = blockIdx.x * 8;
  u32x4 raw[2], gwv[2];
  gload16(raw[0], (s0 + wv) * 2048 + lane * 16, x);
  gload16(raw[1], (s0 + wv) * 2048 + 1024 + lane * 16, x);
  gload16(gwv[0], lane * 16, gw);
  gload16(gwv[1], 1024 + lane * 16, gw);
  warm<7>(w1, (blockIdx.x * 8 + wv) * 7, S_ * F_ * 2 / 1024, lh + 8 * K0_PITCH);
  asm volatile("s_waitcnt vmcnt(7)" : "+v"(raw[0]), "+v"(raw[1]), "+v"(gwv[0]), "+v"(gwv[1]));
  typedef __attribute__((address_space(3))) u32x4* l128;
  *(l128)(size_t)(lx + wv * K0_PITCH + lane * 16) = raw[0];
  *(l128)(size_t)(lx + wv * K0_PITCH + 1024 + lane * 16) = raw[1];
  float v[16], sm = 0.f, gd = 0.f;
#pragma unroll
  for (int i = 0; i < 8; ++i) {
    const unsigned r = raw[i >> 2][i & 3], g = gwv[i >> 2][i & 3];
    v[2 * i] = bf_lo(r); v[2 * i + 1] = bf_hi(r);
    sm += v[2 * i] + v[2 * i + 1];
    gd += v[2 * i] * bf_lo(g) + v[2 * i + 1] * bf_hi(g);
  }
  const float mean = wave_sum(sm) * (1.0f / D_);
  gd = wave_sum(gd);
  float vr = 0.f;
#pragma unroll
  for (int i = 0; i < 16; ++i) { v[i] -= mean; vr += v[i] * v[i]; }
  const float rs = rsqrtf(wave_sum(vr) * (1.0f / D_) + 1e-5f);
#pragma unroll
  for (int h = 0; h < 2; ++h) {
    u32x4 o;
#pragma unroll
    for (int i = 0; i < 4; ++i) o[i] = pack2(v[8 * h + 2 * i] * rs, v[8 * h + 2 * i + 1] * rs);
    *(l128)(size_t)(lh + wv * K0_PITCH + h * 1024 + lane * 16) = o;
  }
  if (lane == 0) ((float*)(ws + WS_G))[s0 + wv] = 1.0f / (1.0f + __expf(-gd));
  bar_lgkm();
#pragma unroll
  for (int h = 0; h < 2; ++h) {
    const int ch = h * 64 + (t >> 3), r = t & 7;
    const u32x4 o = *(l128)(size_t)(lx + r * K0_PITCH + ch * 16);
    *(u32x4*)(ws + WS_XT + ((size_t)((ch >> 2) * 128 + (s0 >> 4)) * 64 + (ch & 3) * 16 + (s0 & 15) + r) * 16) = o;
    const int d0 = wv * 128 + h * 64;
    const bf16x8_t c = lds_tr(lh + ((lane & 15) >> 2) * K0_PITCH + (d0 + (lane >> 4) * 16 + (lane & 3) * 4) * 2, 4 * K0_PITCH);
    const int d = d0 + lane;
    *(bf16x8_t*)(ws + WS_HT + ((size_t)((s0 >> 5) * 64 + (d >> 4)) * 64 + ((s0 & 31) >> 3) * 16 + (d & 15)) * 16) = c;
  }
  wait_vm<0>();
}

DI int sw_std(int r) { return ((r >> 1) & 1) | (((r >> 3) & 1) << 1); }
DI int sw_prm(int r) { return (r >> 1) & 3; }

constexpr int G1_STAGE = 29 * 1024, G1_NS = 5, G1_T = 32;
constexpr int G3_STAGE = 22 * 1024, G3_NS = 6, G3_T = 64;
constexpr int K1_DUMMY = G1_STAGE * G1_NS;
constexpr int K1_LDS = K1_DUMMY + 1024;
static_assert(G3_STAGE * G3_NS <= K1_DUMMY, "");
constexpr int G1_PF = 8, G3_PF = 16;

struct G1Frags { bf16x8_t a[4], ah, b[6], bh; };

struct G1Base { unsigned a, ah, b0, b1, bh; };
template <int SO>
DI void g1_read(G1Frags& f, const G1Base& q) {
#pragma unroll
  for (int p = 0; p < 3; ++p) {
    f.b[2 * p] = lds_tr(q.b0 + (SO + p * 4096), 512);
    f.b[2 * p + 1] = lds_tr(q.b1 + (SO + p * 4096), 512);
  }
#pragma unroll
  for (int m = 0; m < 4; ++m) f.a[m] = lds_b128(q.a + (SO + m * 1024));
  f.bh = lds_tr(q.bh + SO, 512);
  f.ah = lds_b128(q.ah + SO);
}
DI void g1_mma(f32x4 (&acc)[4][6], f32x4& acch, const G1Frags& f) {
  acch = mfma(f.bh, f.ah, acch);
#pragma unroll
  for (int m = 0; m < 4; ++m)
#pragma unroll
    for (int n = 0; n < 6; ++n) acc[m][n] = mfma(f.b[n], f.a[m], acc[m][n]);
}
template <int NMFMA, int NDS>
DI void interleave() {
#pragma unroll
  for (int i = 0; i < NMFMA; ++i) {
    __builtin_amdgcn_sched_group_barrier(0x008, 1, 0);
    if (i < NDS) __builtin_amdgcn_sched_group_barrier(0x100, 1, 0);
    __builtin_amdgcn_sched_group_barrier(0x002, 1, 0);
  }
}

template <int NMFMA, int NDS>
DI void interleave2() {
#pragma unroll
  for (int i = 0; i < NMFMA; ++i) {
    __builtin_amdgcn_sched_group_barrier(0x008, 1, 0);
    if (2 * i < NDS) __builtin_amdgcn_sched_group_barrier(0x100, 2, 0);
    __builtin_amdgcn_sched_group_barrier(0x002, 1, 0);
  }
}

DI void gemm1_attn(int tile_m, int tile_n, const char* __restrict__ wqkv, char* __restrict__ ws, const char* pfb, unsigned pf0, unsigned pfn) {
  const int tid = threadIdx.x, lane = tid & 63;
  const int w = __builtin_amdgcn_readfirstlane(tid >> 6);
  const int wm = w >> 1, wn = w & 1;
  Stamps stp; stp.at(0);
  const char* srcA = ws + WS_XT;
  const char* srcB = wqkv + (size_t)tile_n * 128;
  const unsigned voffA = (unsigned)(tile_m * 16 + 2 * w) * 1024 + lane * 16;
  const unsigned voffH = (unsigned)(tile_m > 0 ? tile_m * 16 - 1 : 0) * 1024 + lane * 16;
  auto voffB = [&](int n) {
    const int r = (n & 3) * 8 + (lane >> 3), c = (lane & 7) ^ (sw_std(r) << 1);
    return (unsigned)(r * (3 * D_ * 2) + (n >> 2) * (D_ * 2) + c * 16);
  };
  const unsigned voffB0 = voffB(w);
  const unsigned voffX = (w < 4) ? voffB(8 + w) : voffH;
  const char* srcX = w < 4 ? srcB : srcA;
  const unsigned strX = w < 4 ? 32 * 3 * D_ * 2 : 128 * 1024, dstX = w < 4 ? 17 * 1024 + (8 + w) * 1024 : 16 * 1024;
  auto issue = [&](int st, unsigned slot) {
    const char* a = srcA + (size_t)st * (128 * 1024);
    const char* b = srcB + (size_t)st * (32 * 3 * D_ * 2);
    glds16(slot + 2 * w * 1024, voffA, a);
    glds16(slot + 2 * w * 1024 + 1024, voffA, a + 1024);
    glds16(slot + 17 * 1024 + w * 1024, voffB0, b);
    glds16(slot + dstX, voffX, srcX + (size_t)st * strX);
  };
  auto waitk = [&](int k) { wait_vm_k<4, G1_NS - 1>(k); };
  auto waitx = [&](int k) { wait_vm_k<4, G1_NS - 1, G1_PF>(k); };
  float gate = ((const float*)(ws + WS_G))[tile_m * 256 + (tid & 255)];
  asm volatile("" : "+v"(gate));
  const unsigned a_base = wm * 4096 + lane * 16;
  const unsigned ah_base = 16 * 1024 + lane * 16;
  const int trr = (lane >> 4) * 8 + ((lane & 15) >> 2), trp = lane & 3;
  auto tr_addr = [&](int jb) { return (unsigned)(17 * 1024 + trr * 128 + ((jb ^ sw_std(trr)) * 32) + trp * 8); };
  const unsigned b0 = tr_addr(2 * wn), b1 = tr_addr(2 * wn + 1);
  const unsigned bh = tr_addr(2 * wn + (wm & 1)) + (1 + (wm >> 1)) * 4096;

  f32x4 acc[4][6], acch = {0.f, 0.f, 0.f, 0.f};
#pragma unroll
  for (int m = 0; m < 4; ++m)
#pragma unroll
    for (int n = 0; n < 6; ++n) acc[m][n] = f32x4{0.f, 0.f, 0.f, 0.f};

#pragma unroll
  for (int s = 0; s < G1_NS; ++s) issue(s, s * G1_STAGE);
  waitk(G1_NS - 1);
  bar_raw();
  stp.at(1);
  G1Base qb[3];
#pragma unroll
  for (int i = 0; i < 3; ++i) {
    const unsigned o = i * 2 * G1_STAGE;
    qb[i] = G1Base{pin(a_base + o), pin(ah_base + o), pin(b0 + o), pin(b1 + o), pin(bh + o)};
  }
  G1Frags fx, fy;
  g1_read<0>(fx, qb[0]);
  auto step = [&](auto slc, auto tail, int t, G1Frags& fc, G1Frags& fn) {
    constexpr int SL = decltype(slc)::v, NX = (SL + 1) % G1_NS;
    if constexpr (decltype(tail)::v == 0) {
      waitk(G1_NS - 2);
      bar_lgkm();
      issue(t + G1_NS, SL * G1_STAGE);
    } else {
      if (t + G1_NS <= G1_T) waitk(G1_NS - 2);
      else waitx(G1_T - 2 - t);
      bar_lgkm();
      if (t + G1_NS < G1_T) issue(t + G1_NS, SL * G1_STAGE);
      else if (t + G1_NS == G1_T) warm<G1_PF>(pfb, pf0 + w * G1_PF, pfn, K1_DUMMY);
    }
    __builtin_amdgcn_sched_barrier(0);
    g1_read<(NX & 1) * G1_STAGE>(fn, qb[NX >> 1]);
    g1_mma(acc, acch, fc);
    interleave<25, 19>();
    __builtin_amdgcn_sched_barrier(0);
    asm volatile("" : "+v"(acch));
  };
  auto ten = [&](auto tail, int t) {
    step(IC<0>{}, tail, t, fx, fy);     step(IC<1>{}, tail, t + 1, fy, fx);
    step(IC<2>{}, tail, t + 2, fx, fy); step(IC<3>{}, tail, t + 3, fy, fx);
    step(IC<4>{}, tail, t + 4, fx, fy); step(IC<0>{}, tail, t + 5, fy, fx);
    step(IC<1>{}, tail, t + 6, fx, fy); step(IC<2>{}, tail, t + 7, fy, fx);
    step(IC<3>{}, tail, t + 8, fx, fy); step(IC<4>{}, tail, t + 9, fy, fx);
  };
  for (int t = 0; t < 20; t += 10) ten(IC<0>{}, t);
  ten(IC<1>{}, 20);
  step(IC<0>{}, IC<1>{}, 30, fx, fy);
  g1_mma(acc, acch, fy);
  bar_lgkm();
  stp.at(2);

  constexpr int PL = 272 * 16;
  const int tid2 = pin(threadIdx.x), ln = tid2 & 63;
  const int hd = tid2 >> 8, row = tid2 & 255, s = tile_m * 256 + row;
  {
    const unsigned wbase = wn * (12 * PL) + (ln >> 5) * PL + ((ln >> 4) & 1) * 8 + (ln & 15) * 16;
#pragma unroll
    for (int m = 0; m < 4; ++m)
#pragma unroll
      for (int n = 0; n < 6; ++n) {
        u32x2 o = {pack2(acc[m][n][0], acc[m][n][1]), pack2(acc[m][n][2], acc[m][n][3])};
        *(__attribute__((address_space(3))) u32x2*)(size_t)(wbase + n * 2 * PL + (16 + wm * 64 + m * 16) * 16) = o;
      }
    u32x2 o = {pack2(acch[0], acch[1]), pack2(acch[2], acch[3])};
    *(__attribute__((address_space(3))) u32x2*)(size_t)(wbase + (2 + wm) * 2 * PL) = o;
  }
  bar_lgkm();
  {
    const unsigned rbase = hd * (12 * PL) + (16 + row) * 16;
    auto ld = [&](int mat, int c, int back) {
      return *(const __attribute__((address_space(3))) u32x4*)(size_t)(rbase + (mat * 4 + c) * PL - back * 32);
    };
    u32x4 q[4];
#pragma unroll
    for (int c = 0; c < 4; ++c) q[c] = ld(0, c, 0);
    float lg[8];
#pragma unroll
    for (int wd = 0; wd < 8; ++wd) {
      float a = 0.f;
#pragma unroll
      for (int c = 0; c < 4; ++c) {
        u32x4 kk = ld(1, c, wd);
#pragma unroll
        for (int i = 0; i < 4; ++i)
          a = dot2(q[c][i], kk[i], a);
      }
      lg[wd] = (s - 2 * wd >= 0) ? a * 0.17677669529663687f : -1e9f;
    }
    float mx = lg[0];
#pragma unroll
    for (int wd = 1; wd < 8; ++wd) mx = fmaxf(mx, lg[wd]);
    float sum = 0.f;
#pragma unroll
    for (int wd = 0; wd < 8; ++wd) { lg[wd] = __expf(lg[wd] - mx); sum += lg[wd]; }
    const float sc = gate / sum;
    float o[32];
#pragma unroll
    for (int i = 0; i < 32; ++i) o[i] = 0.f;
#pragma unroll
    for (int wd = 0; wd < 8; ++wd) {
      const bool ok = (s - 2 * wd >= 0);
      const float p = lg[wd] * sc;
#pragma unroll
      for (int c = 0; c < 4; ++c) {
        u32x4 vv = ld(2, c, ok ? wd : 0);
#pragma unroll
        for (int i = 0; i < 4; ++i) {
          o[c * 8 + 2 * i] += p * bf_lo(vv[i]);
          o[c * 8 + 2 * i + 1] += p * bf_hi(vv[i]);
        }
      }
    }
    u32x4* dst = (u32x4*)(ws + WS_OT) + ((size_t)((tile_n * 2 + hd) * 128 + (s >> 4)) * 64 + (s & 15));
#pragma unroll
    for (int qq = 0; qq < 4; ++qq) {
      u32x4 r = {pack2(o[4 * qq], o[4 * qq + 1]), pack2(o[4 * qq + 2], o[4 * qq + 3]),
                 pack2(o[16 + 4 * qq], o[16 + 4 * qq + 1]), pack2(o[16 + 4 * qq + 2], o[16 + 4 * qq + 3])};
      dst[qq * 16] = r;
    }
  }
  wait_vm<0>();
  stp.at(3); stp.out(ws, 0);
}

struct G3Frags { bf16x8_t h[4], w[7]; };

DI float gelu_tanh(float u) {
  const float c0 = -2.0f * 0.7978845608028654f * 1.4426950408889634f;
  const float c1 = c0 * 0.044715f;
  const float e = __builtin_amdgcn_exp2f(u * __builtin_fmaf(u * u, c1, c0));
  return u * __builtin_amdgcn_rcpf(1.0f + e);
}

DI void gemm3_gelu(int tile_d, int tile_f, const char* __restrict__ w1, const char* __restrict__ b1, char* __restrict__ ws, const char* pfb, unsigned pf0, unsigned pfn) {
  const int tid = threadIdx.x, lane = tid & 63;
  const int w = __builtin_amdgcn_readfirstlane(tid >> 6);
  const int odd = tile_f & 1;
  Stamps stp; stp.at(0);
  if (w >= 4) {
    const int lw = w - 4;
    const char* srcA = ws + WS_HT;
    const char* srcB = w1 + (size_t)tile_f * 448;
    const unsigned voffA = (unsigned)(tile_d * 8 + 2 * lw) * 1024 + lane * 16;
    const int r = lw * 8 + (lane >> 3), c = (lane & 7) ^ (sw_std(r) << 1);
    const unsigned voffB = (unsigned)(r * (F_ * 2) + odd * 64 + c * 16);
    const int hg = (lw & 1) * 2 + ((lane >> 4) & 1), hr = hg * 8 + (lane >> 5) * 4 + ((lane >> 2) & 3);
    const int hc = (lane & 3) ^ ((hg & 1) << 1);
    const unsigned voffH = (unsigned)(hr * (F_ * 2) + (odd ? 0 : 384) + hc * 16);
    auto issue = [&](int st, unsigned slot) {
      const char* a = srcA + (size_t)st * (64 * 1024);
      const char* b = srcB + (size_t)st * (32 * F_ * 2);
      glds16(slot + 2 * lw * 1024, voffA, a);
      glds16(slot + 2 * lw * 1024 + 1024, voffA + 1024, a);
      glds16(slot + 8192 + lw * 1024, voffB, b);
      glds16(slot + 8192 + 4096 + lw * 1024, voffB + 128, b);
      glds16(slot + 8192 + 8192 + lw * 1024, voffB + 256, b);
      if (lw < 2) glds16(slot + 8192 + 12288 + lw * 1024, voffH, b);
    };
    auto waitk = [&](int k) {
      if (lw < 2) wait_vm_k<6, G3_NS - 1>(k);
      else wait_vm_k<5, G3_NS - 1>(k);
    };
    auto waitx = [&](int k) {
      if (lw < 2) wait_vm_k<6, G3_NS - 1, G3_PF>(k);
      else wait_vm_k<5, G3_NS - 1, G3_PF>(k);
    };
#pragma unroll
    for (int s = 0; s < G3_NS; ++s) issue(s, s * G3_STAGE);
    waitk(G3_NS - 1);
    bar_raw();
    unsigned cur = 0;
    for (int t = 0; t < G3_T - G3_NS; ++t) {
      waitk(G3_NS - 2);
      bar_raw();
      issue(t + G3_NS, cur);
      cur += G3_STAGE;
      if (cur == G3_NS * G3_STAGE) cur = 0;
    }
    waitk(G3_NS - 2);
    bar_raw();
    warm<G3_PF>(pfb, pf0 + lw * G3_PF, pfn, K1_DUMMY);
#pragma unroll
    for (int k = G3_NS - 3; k >= 0; --k) {
      waitx(k);
      bar_raw();
    }
    wait_vm<0>();
    return;
  }
  const int wd = w >> 1, wf = w & 1;
  const unsigned h_base = wd * 4096 + lane * 16;
  const int g = lane >> 4, qq = (lane & 15) >> 2, trp = lane & 3, trr = g * 8 + qq;
  unsigned wb[7];
  int fcol[7];
#pragma unroll
  for (int i = 0; i < 7; ++i) {
    const int lt = wf * 7 + i;
    const unsigned pa = 8192 + (lt >> 2) * 4096 + trr * 128 + (((lt & 3) ^ sw_std(trr)) * 32) + trp * 8;
    const unsigned ha = 8192 + 12288 + (g >> 1) * 1024 + (g & 1) * 256 + qq * 64 + (((lt & 1) ^ (g & 1)) * 32) + trp * 8;
    wb[i] = lt < 12 ? pa : ha;
    fcol[i] = tile_f * 224 + (odd ? (lt < 12 ? 32 + 16 * lt : 16 * (lt - 12)) : 16 * lt);
  }
  f32x4 acc[7][4];
#pragma unroll
  for (int i = 0; i < 7; ++i) {
    const u32x2 bias = *(const u32x2*)(b1 + (size_t)(fcol[i] + 4 * g) * 2);
    const unsigned b0 = bias[0], b1v = bias[1];
#pragma unroll
    for (int m = 0; m < 4; ++m) acc[i][m] = f32x4{bf_lo(b0), bf_hi(b0), bf_lo(b1v), bf_hi(b1v)};
  }
  unsigned hq[2], wq[2][7];
#pragma unroll
  for (int c = 0; c < 2; ++c) {
    hq[c] = pin(h_base + c * 3 * G3_STAGE);
#pragma unroll
    for (int i = 0; i < 7; ++i) wq[c][i] = pin(wb[i] + c * 3 * G3_STAGE);
  }
  auto rd = [&](G3Frags& f, auto slc) {
    constexpr int C = decltype(slc)::v / 3, SO = (decltype(slc)::v % 3) * G3_STAGE;
#pragma unroll
    for (int m = 0; m < 4; ++m) f.h[m] = lds_b128(hq[C] + (SO + m * 1024));
#pragma unroll
    for (int i = 0; i < 7; ++i) f.w[i] = lds_tr(wq[C][i] + SO, 512);
  };
  auto mm = [&](const G3Frags& f) {
#pragma unroll
    for (int i = 0; i < 7; ++i)
#pragma unroll
      for (int m = 0; m < 4; ++m) acc[i][m] = mfma(f.w[i], f.h[m], acc[i][m]);
  };
  G3Frags fx, fy;
  bar_raw();
  stp.at(1);
  rd(fx, IC<0>{});
  auto step = [&](auto nx, G3Frags& fc, G3Frags& fn) {
    bar_lgkm();
    __builtin_amdgcn_sched_barrier(0);
    rd(fn, nx);
    mm(fc);
    interleave<28, 18>();
    __builtin_amdgcn_sched_barrier(0);
  };
  for (int t = 0; t < 60; t += 6) {
    step(IC<1>{}, fx, fy); step(IC<2>{}, fy, fx); step(IC<3>{}, fx, fy);
    step(IC<4>{}, fy, fx); step(IC<5>{}, fx, fy); step(IC<0>{}, fy, fx);
  }
  step(IC<1>{}, fx, fy); step(IC<2>{}, fy, fx); step(IC<3>{}, fx, fy);
  mm(fy);
  stp.at(2);
  char* ut = ws + WS_UT;
  const int ln = pin(threadIdx.x) & 63;
#pragma unroll
  for (int m = 0; m < 4; ++m) {
    u32x2 pk[7];
#pragma unroll
    for (int i = 0; i < 7; ++i) {
      const float v0 = gelu_tanh(acc[i][m][0]), v1 = gelu_tanh(acc[i][m][1]);
      const float v2 = gelu_tanh(acc[i][m][2]), v3 = gelu_tanh(acc[i][m][3]);
      pk[i] = u32x2{pack2(v0, v1), pack2(v2, v3)};
    }
    const unsigned rbo = (unsigned)(tile_d * 8 + wd * 4 + m) * 1024 + ln * 16;
    if (wf == 0) {
#pragma unroll
      for (int p = 0; p < 3; ++p)
        *(u32x4*)(ut + (size_t)(fcol[2 * p] >> 5) * 65536 + rbo) = u32x4{pk[2 * p][0], pk[2 * p][1], pk[2 * p + 1][0], pk[2 * p + 1][1]};
      *(u32x2*)(ut + (size_t)(fcol[6] >> 5) * 65536 + rbo) = pk[6];
    } else {
      *(u32x2*)(ut + (size_t)(fcol[0] >> 5) * 65536 + rbo + 8) = pk[0];
#pragma unroll
      for (int p = 0; p < 3; ++p)
        *(u32x4*)(ut + (size_t)(fcol[2 * p + 1] >> 5) * 65536 + rbo) =
            u32x4{pk[2 * p + 1][0], pk[2 * p + 1][1], pk[2 * p + 2][0], pk[2 * p + 2][1]};
    }
  }
  stp.at(3); stp.out(ws, 0);
}

__global__ __launch_bounds__(512, 2) void k1_gemms(const char* __restrict__ wqkv, const char* __restrict__ w1,
                                                   const char* __restrict__ b1, char* __restrict__ ws,
                                                   const char* __restrict__ w2, const char* __restrict__ wo) {
  const int b = blockIdx.x, xcd = b & 7, j = b >> 3;
  const char* pfb = b < 224 ? w2 : wo;
  const unsigned pf0 = (b < 224 ? b : b - 224) * 64, pfn = b < 224 ? F_ * S_ * 2 / 1024 : D_ * D_ * 2 / 1024;
  if (xcd < 4) gemm1_attn((xcd & 1) * 4 + (j & 3), (xcd >> 1) * 8 + (j >> 2), wqkv, ws, pfb, pf0, pfn);
  else gemm3_gelu(j & 7, (xcd - 4) * 4 + (j >> 3), w1, b1, ws, pfb, pf0, pfn);
}

constexpr int K2_STAGE = 24 * 1024, K2_NS = 6, K2_T1 = 56, K2_T = 72;
constexpr int K2_LDS = K2_STAGE * K2_NS;

struct K2Frags { bf16x8_t s[2][4], d[2][2]; };

__global__ __launch_bounds__(512, 2) void k2_out(const char* __restrict__ wo, const char* __restrict__ w2,
                                                 const char* __restrict__ b2, const char* __restrict__ ws,
                                                 char* __restrict__ out) {
  const int b = blockIdx.x, xcd = b & 7, j = b >> 3;
  const int tile_s = (xcd >> 1) * 4 + (j & 3), tile_d = (xcd & 1) * 8 + (j >> 2);
  const int tid = threadIdx.x, lane = tid & 63;
  const int w = __builtin_amdgcn_readfirstlane(tid >> 6);
  Stamps stp; stp.at(0);
  if (w >= 4) {
    const int lw = w - 4;
    const int r = lw * 8 + (lane >> 3), c = (lane & 7) ^ (sw_prm(r) << 1);
    const unsigned voffW2 = (unsigned)(r * (S_ * 2) + tile_s * 256 + c * 16);
    const unsigned voffU = (unsigned)(tile_d * 4 + lw) * 1024 + lane * 16;
    const unsigned voffO = (unsigned)(tile_s * 8 + 2 * lw) * 1024 + lane * 16;
    const unsigned voffWo = (unsigned)(r * (D_ * 2) + tile_d * 128 + c * 16);
    auto issue = [&](int st, unsigned slot) {
      if (st < K2_T1) {
        const char* a = w2 + (size_t)st * (64 * S_ * 2);
        const char* u = ws + WS_UT + (size_t)st * (2 * 65536);
#pragma unroll
        for (int kk = 0; kk < 2; ++kk) {
          glds16(slot + kk * 8192 + lw * 1024, voffW2 + kk * (32 * S_ * 2), a);
          glds16(slot + kk * 8192 + 4096 + lw * 1024, voffW2 + kk * (32 * S_ * 2) + 128, a);
          glds16(slot + 16384 + kk * 4096 + lw * 1024, voffU + kk * 65536, u);
        }
      } else {
        const char* o = ws + WS_OT + (size_t)(st - K2_T1) * (2 * 131072);
        const char* a = wo + (size_t)(st - K2_T1) * (64 * D_ * 2);
#pragma unroll
        for (int kk = 0; kk < 2; ++kk) {
          glds16(slot + kk * 8192 + 2 * lw * 1024, voffO + kk * 131072, o);
          glds16(slot + kk * 8192 + 2 * lw * 1024 + 1024, voffO + kk * 131072 + 1024, o);
          glds16(slot + 16384 + kk * 4096 + lw * 1024, voffWo + kk * (32 * D_ * 2), a);
        }
      }
    };
#pragma unroll
    for (int s = 0; s < K2_NS; ++s) issue(s, s * K2_STAGE);
    wait_vm<6 * (K2_NS - 1)>();
    bar_raw();
    unsigned cur = 0;
    for (int t = 0; t < K2_T - 1; ++t) {
      wait_vm_k<6, K2_NS - 2>(min(K2_NS - 2, K2_T - 2 - t));
      bar_raw();
      if (t + K2_NS < K2_T) issue(t + K2_NS, cur);
      cur += K2_STAGE;
      if (cur == K2_NS * K2_STAGE) cur = 0;
    }
    return;
  }
  const int ws_ = w >> 1, wd = w & 1;
  const int g = lane >> 4, trr = g * 4 + ((lane & 15) >> 2), trp = lane & 3;
  unsigned ta[4];
#pragma unroll
  for (int i = 0; i < 4; ++i) ta[i] = trr * 128 + ((i ^ sw_prm(trr)) * 32) + trp * 8;
  const unsigned tw0 = trr * 128 + (((2 * wd) ^ sw_prm(trr)) * 32) + trp * 8 + 16384;
  const unsigned tw1 = trr * 128 + (((2 * wd + 1) ^ sw_prm(trr)) * 32) + trp * 8 + 16384;
  const unsigned lin = lane * 16;
  float gt[4], bb[4];
#pragma unroll
  for (int i = 0; i < 4; ++i) {
    const int s = tile_s * 128 + ws_ * 64 + i * 16 + (lane & 15);
    gt[i] = ((const float*)(ws + WS_G))[s];
    bb[i] = bf_lo(((const unsigned short*)b2)[s]);
  }
  f32x4 acc[4][2];
#pragma unroll
  for (int i = 0; i < 4; ++i)
#pragma unroll
    for (int n = 0; n < 2; ++n) acc[i][n] = f32x4{0.f, 0.f, 0.f, 0.f};
  auto rd1 = [&](K2Frags& f, unsigned so) {
#pragma unroll
    for (int kk = 0; kk < 2; ++kk) {
#pragma unroll
      for (int n = 0; n < 2; ++n) f.d[kk][n] = lds_b128(lin + so + 16384 + kk * 4096 + (wd * 2 + n) * 1024);
#pragma unroll
      for (int i = 0; i < 4; ++i) f.s[kk][i] = lds_tr(ta[i] + so + kk * 8192 + ws_ * 4096, 2048);
    }
  };
  auto rd2 = [&](K2Frags& f, unsigned so) {
#pragma unroll
    for (int kk = 0; kk < 2; ++kk) {
      f.d[kk][0] = lds_tr(tw0 + so + kk * 4096, 2048);
      f.d[kk][1] = lds_tr(tw1 + so + kk * 4096, 2048);
#pragma unroll
      for (int i = 0; i < 4; ++i) f.s[kk][i] = lds_b128(lin + so + kk * 8192 + (ws_ * 4 + i) * 1024);
    }
  };
  auto mm = [&](const K2Frags& f) {
#pragma unroll
    for (int kk = 0; kk < 2; ++kk)
#pragma unroll
      for (int i = 0; i < 4; ++i)
#pragma unroll
        for (int n = 0; n < 2; ++n) acc[i][n] = mfma(f.d[kk][n], f.s[kk][i], acc[i][n]);
  };
#pragma unroll
  for (int i = 0; i < 4; ++i) asm volatile("" : "+v"(gt[i]), "+v"(bb[i]));
  K2Frags fx, fy;
  bar_raw();
  stp.at(1);
  rd1(fx, 0);
  unsigned cur = 0;
  auto adv = [&]() {
    bar_lgkm();
    cur += K2_STAGE;
    if (cur == K2_NS * K2_STAGE) cur = 0;
    __builtin_amdgcn_sched_barrier(0);
  };
  auto step1 = [&](K2Frags& fc, K2Frags& fn) { adv(); rd1(fn, cur); mm(fc); interleave2<16, 20>(); __builtin_amdgcn_sched_barrier(0); };
  auto step2 = [&](K2Frags& fc, K2Frags& fn) { adv(); rd2(fn, cur); mm(fc); interleave2<16, 16>(); __builtin_amdgcn_sched_barrier(0); };
  for (int t = 0; t < K2_T1 - 2; t += 2) {
    step1(fx, fy);
    step1(fy, fx);
  }
  step1(fx, fy);
  step2(fy, fx);
#pragma unroll
  for (int i = 0; i < 4; ++i)
#pragma unroll
    for (int n = 0; n < 2; ++n)
#pragma unroll
      for (int e = 0; e < 4; ++e) acc[i][n][e] = (acc[i][n][e] + bb[i]) * (1.0f - gt[i]);
  for (int t = K2_T1; t < K2_T - 2; t += 2) {
    step2(fx, fy);
    step2(fy, fx);
  }
  step2(fx, fy);
  mm(fy);
  stp.at(2);
#pragma unroll
  for (int i = 0; i < 4; ++i)
#pragma unroll
    for (int n = 0; n < 2; ++n) {
      const int s = tile_s * 128 + ws_ * 64 + i * 16 + (lane & 15);
      const int d = tile_d * 64 + wd * 32 + n * 16 + g * 4;
      *(u32x2*)(out + ((size_t)s * D_ + d) * 2) = u32x2{pack2(acc[i][n][0], acc[i][n][1]), pack2(acc[i][n][2], acc[i][n][3])};
    }
  stp.at(3); stp.out(ws, 1);
}

extern "C" void kernel_launch(void* const* d_in, const int* in_sizes, int n_in, void* d_out, int out_size, void* d_ws,
                              size_t ws_size, hipStream_t stream) {
  const char* x = (const char*)d_in[0];
  const char* wqkv = (const char*)d_in[1];
  const char* wo = (const char*)d_in[2];
  const char* w1 = (const char*)d_in[4];
  const char* b1 = (const char*)d_in[5];
  const char* w2 = (const char*)d_in[6];
  const char* b2 = (const char*)d_in[7];
  const char* gw = (const char*)d_in[8];
  char* ws = (char*)d_ws;
  (void)hipFuncSetAttribute((const void*)k1_gemms, hipFuncAttributeMaxDynamicSharedMemorySize, K1_LDS);
  (void)hipFuncSetAttribute((const void*)k2_out, hipFuncAttributeMaxDynamicSharedMemorySize, K2_LDS);
  k0_prep<<<256, 512, 0, stream>>>(x, gw, ws, w1);
  k1_gemms<<<256, 512, K1_LDS, stream>>>(wqkv, w1, b1, ws, w2, wo);
  k2_out<<<256, 512, K2_LDS, stream>>>(wo, w2, b2, ws, (char*)d_out);
}
```

```cpp
#include <hip/hip_runtime.h>
#include <hip/hip_bf16.h>

typedef __attribute__((ext_vector_type(2))) __bf16 bf16x2_t;
typedef __attribute__((ext_vector_type(8))) __bf16 bf16x8_t;
typedef __attribute__((ext_vector_type(4))) short s16x4;
typedef __attribute__((ext_vector_type(8))) short s16x8;
typedef __attribute__((ext_vector_type(4))) float f32x4;
typedef __attribute__((ext_vector_type(2))) float f32x2;
typedef __attribute__((ext_vector_type(4))) unsigned u32x4;
typedef __attribute__((ext_vector_type(2))) unsigned u32x2;
typedef __attribute__((address_space(3))) char* lds_ptr;

#define DI __device__ __forceinline__

constexpr int S_ = 2048, D_ = 1024, F_ = 3584;

constexpr size_t WS_XT = 0;
constexpr size_t WS_HT = WS_XT + (size_t)S_ * D_ * 2;
constexpr size_t WS_OT = WS_HT + (size_t)S_ * D_ * 2;
constexpr size_t WS_UT = WS_OT + (size_t)S_ * D_ * 2;
constexpr size_t WS_G = WS_UT + (size_t)D_ * F_ * 2;
constexpr size_t WS_P = WS_G + 8192;
constexpr size_t WS_FLAG = WS_P + (size_t)S_ * D_ * 4;

struct Stamps {
  DI void at(int) {}
  DI void out(const char*, int) {}
};

DI unsigned pack2(float a, float b) {
  f32x2 f = {a, b};
  return __builtin_bit_cast(unsigned, __builtin_convertvector(f, bf16x2_t));
}
DI float bf_lo(unsigned u) { return __builtin_bit_cast(float, u << 16); }
DI float bf_hi(unsigned u) { return __builtin_bit_cast(float, u & 0xffff0000u); }

DI float dot2(unsigned a, unsigned b, float c) {
  return __builtin_amdgcn_fdot2_f32_bf16(__builtin_bit_cast(bf16x2_t, a), __builtin_bit_cast(bf16x2_t, b), c, false);
}

DI void glds16(unsigned lds_dst, unsigned voff, const void* sbase) {
  asm volatile("s_mov_b32 m0, %0\n\ts_nop 0\n\tglobal_load_lds_dwordx4 %1, %2" ::"s"(lds_dst), "v"(voff), "s"(sbase)
               : "memory");
}
template <int N>
DI void wait_vm() {
  asm volatile("s_waitcnt vmcnt(%0)" ::"i"(N) : "memory");
}
DI void bar_raw() { asm volatile("s_barrier" ::: "memory"); }
DI void bar_lgkm() {
  __builtin_amdgcn_s_waitcnt(0xC07F);
  asm volatile("s_barrier" ::: "memory");
}

DI bf16x8_t lds_b128(unsigned addr) {
  return *(const __attribute__((address_space(3))) bf16x8_t*)(size_t)addr;
}
DI bf16x8_t lds_tr(unsigned addr, unsigned off2) {
  s16x4 lo = __builtin_amdgcn_ds_read_tr16_b64_v4i16((__attribute__((address_space(3))) s16x4*)(size_t)addr);
  s16x4 hi = __builtin_amdgcn_ds_read_tr16_b64_v4i16((__attribute__((address_space(3))) s16x4*)(size_t)(addr + off2));
  s16x8 r = __builtin_shufflevector(lo, hi, 0, 1, 2, 3, 4, 5, 6, 7);
  return __builtin_bit_cast(bf16x8_t, r);
}
DI f32x4 mfma(bf16x8_t a, bf16x8_t b, f32x4 c) { return __builtin_amdgcn_mfma_f32_16x16x32_bf16(a, b, c, 0, 0, 0); }

template <int V> struct IC { static constexpr int v = V; };
DI unsigned pin(unsigned v) { asm volatile("" : "+v"(v)); return v; }

template <int N, int KMAX, int X = 0>
DI void wait_vm_k(int k) {
  if (k >= KMAX) wait_vm<N * KMAX + X>();
  else if constexpr (KMAX > 0) wait_vm_k<N, KMAX - 1, X>(k);
}

template <int NP>
DI void warm(const char* base, unsigned piece0, unsigned npieces, unsigned lds_dummy) {
  const unsigned lane16 = (threadIdx.x & 63) * 16;
#pragma unroll
  for (int i = 0; i < NP; ++i) {
    const unsigned pc = min(piece0 + i, npieces - 1);
    glds16(lds_dummy, pc * 1024 + lane16, base);
  }
}

template <int CTRL>
DI float dpp_add(float v) {
  return v + __builtin_bit_cast(float, __builtin_amdgcn_update_dpp(0, __builtin_bit_cast(int, v), CTRL, 0xf, 0xf, true));
}
DI float rdlane(float v, int l) { return __builtin_bit_cast(float, __builtin_amdgcn_readlane(__builtin_bit_cast(int, v), l)); }
DI float wave_sum(float v) {
  v = dpp_add<0xB1>(v);
  v = dpp_add<0x4E>(v);
  v = dpp_add<0x141>(v);
  v = dpp_add<0x140>(v);
  return (rdlane(v, 0) + rdlane(v, 16)) + (rdlane(v, 32) + rdlane(v, 48));
}
DI void gload16(u32x4& d, unsigned voff, const void* sbase) {
  asm volatile("global_load_dwordx4 %0, %1, %2" : "=v"(d) : "v"(voff), "s"(sbase) : "memory");
}

constexpr int K0_PITCH = 2048 + 16;
__global__ __launch_bounds__(512) void k0_prep(const char* __restrict__ x, const char* __restrict__ gw, char* __restrict__ ws,
                                               const char* __restrict__ w1) {
  __shared__ __attribute__((aligned(16))) char lds[2 * 8 * K0_PITCH + 1024];
  const unsigned lx = (unsigned)(size_t)(lds_ptr)lds, lh = lx + 8 * K0_PITCH;
  const int t = threadIdx.x, lane = t & 63;
  const int wv = __builtin_amdgcn_readfirstlane(t >> 6);
  const int s0 = blockIdx.x * 8;
  u32x4 raw[2], gwv[2];
  gload16(raw[0], (s0 + wv) * 2048 + lane * 16, x);
  gload16(raw[1], (s0 + wv) * 2048 + 1024 + lane * 16, x);
  gload16(gwv[0], lane * 16, gw);
  gload16(gwv[1], 1024 + lane * 16, gw);
  warm<7>(w1, (blockIdx.x * 8 + wv) * 7, S_ * F_ * 2 / 1024, lh + 8 * K0_PITCH);
  asm volatile("s_waitcnt vmcnt(7)" : "+v"(raw[0]), "+v"(raw[1]), "+v"(gwv[0]), "+v"(gwv[1]));
  typedef __attribute__((address_space(3))) u32x4* l128;
  *(l128)(size_t)(lx + wv * K0_PITCH + lane * 16) = raw[0];
  *(l128)(size_t)(lx + wv * K0_PITCH + 1024 + lane * 16) = raw[1];
  float v[16], sm = 0.f, gd = 0.f;
#pragma unroll
  for (int i = 0; i < 8; ++i) {
    const unsigned r = raw[i >> 2][i & 3], g = gwv[i >> 2][i & 3];
    v[2 * i] = bf_lo(r); v[2 * i + 1] = bf_hi(r);
    sm += v[2 * i] + v[2 * i + 1];
    gd += v[2 * i] * bf_lo(g) + v[2 * i + 1] * bf_hi(g);
  }
  const float mean = wave_sum(sm) * (1.0f / D_);
  gd = wave_sum(gd);
  float vr = 0.f;
#pragma unroll
  for (int i = 0; i < 16; ++i) { v[i] -= mean; vr += v[i] * v[i]; }
  const float rs = rsqrtf(wave_sum(vr) * (1.0f / D_) + 1e-5f);
#pragma unroll
  for (int h = 0; h < 2; ++h) {
    u32x4 o;
#pragma unroll
    for (int i = 0; i < 4; ++i) o[i] = pack2(v[8 * h + 2 * i] * rs, v[8 * h + 2 * i + 1] * rs);
    *(l128)(size_t)(lh + wv * K0_PITCH + h * 1024 + lane * 16) = o;
  }
  if (lane == 0) ((float*)(ws + WS_G))[s0 + wv] = 1.0f / (1.0f + __expf(-gd));
  if (t < 2) *(unsigned*)(ws + WS_FLAG + (size_t)(blockIdx.x * 2 + t) * 128) = 0u;
  bar_lgkm();
#pragma unroll
  for (int h = 0; h < 2; ++h) {
    const int ch = h * 64 + (t >> 3), r = t & 7;
    const u32x4 o = *(l128)(size_t)(lx + r * K0_PITCH + ch * 16);
    *(u32x4*)(ws + WS_XT + ((size_t)((ch >> 2) * 128 + (s0 >> 4)) * 64 + (ch & 3) * 16 + (s0 & 15) + r) * 16) = o;
    const int d0 = wv * 128 + h * 64;
    const bf16x8_t c = lds_tr(lh + ((lane & 15) >> 2) * K0_PITCH + (d0 + (lane >> 4) * 16 + (lane & 3) * 4) * 2, 4 * K0_PITCH);
    const int d = d0 + lane;
    *(bf16x8_t*)(ws + WS_HT + ((size_t)((s0 >> 5) * 64 + (d >> 4)) * 64 + ((s0 & 31) >> 3) * 16 + (d & 15)) * 16) = c;
  }
  wait_vm<0>();
}

DI int sw_std(int r) { return ((r >> 1) & 1) | (((r >> 3) & 1) << 1); }
DI int sw_prm(int r) { return (r >> 1) & 3; }

constexpr int G1_STAGE = 29 * 1024, G1_NS = 5, G1_T = 32;
constexpr int G3_STAGE = 22 * 1024, G3_NS = 6, G3_T = 64;
constexpr int K1_DUMMY = G1_STAGE * G1_NS;
constexpr int K1_LDS = K1_DUMMY + 1024;
static_assert(G3_STAGE * G3_NS <= K1_DUMMY, "");
constexpr int G1_PF = 8, G3_PF = 16;

struct G1Frags { bf16x8_t a[4], ah, b[6], bh; };

struct G1Base { unsigned a, ah, b0, b1, bh; };
template <int SO>
DI void g1_read(G1Frags& f, const G1Base& q) {
#pragma unroll
  for (int p = 0; p < 3; ++p) {
    f.b[2 * p] = lds_tr(q.b0 + (SO + p * 4096), 512);
    f.b[2 * p + 1] = lds_tr(q.b1 + (SO + p * 4096), 512);
  }
#pragma unroll
  for (int m = 0; m < 4; ++m) f.a[m] = lds_b128(q.a + (SO + m * 1024));
  f.bh = lds_tr(q.bh + SO, 512);
  f.ah = lds_b128(q.ah + SO);
}
DI void g1_mma(f32x4 (&acc)[4][6], f32x4& acch, const G1Frags& f) {
  acch = mfma(f.bh, f.ah, acch);
#pragma unroll
  for (int m = 0; m < 4; ++m)
#pragma unroll
    for (int n = 0; n < 6; ++n) acc[m][n] = mfma(f.b[n], f.a[m], acc[m][n]);
}
template <int NMFMA, int NDS>
DI void interleave() {
#pragma unroll
  for (int i = 0; i < NMFMA; ++i) {
    __builtin_amdgcn_sched_group_barrier(0x008, 1, 0);
    if (i < NDS) __builtin_amdgcn_sched_group_barrier(0x100, 1, 0);
    __builtin_amdgcn_sched_group_barrier(0x002, 1, 0);
  }
}

template <int NMFMA, int NDS>
DI void interleave2() {
#pragma unroll
  for (int i = 0; i < NMFMA; ++i) {
    __builtin_amdgcn_sched_group_barrier(0x008, 1, 0);
    if (2 * i < NDS) __builtin_amdgcn_sched_group_barrier(0x100, 2, 0);
    __builtin_amdgcn_sched_group_barrier(0x002, 1, 0);
  }
}

DI void gemm1_attn(int tile_m, int tile_n, const char* __restrict__ wqkv, char* __restrict__ ws, const char* pfb, unsigned pf0, unsigned pfn) {
  const int tid = threadIdx.x, lane = tid & 63;
  const int w = __builtin_amdgcn_readfirstlane(tid >> 6);
  const int wm = w >> 1, wn = w & 1;
  Stamps stp; stp.at(0);
  const char* srcA = ws + WS_XT;
  const char* srcB = wqkv + (size_t)tile_n * 128;
  const unsigned voffA = (unsigned)(tile_m * 16 + 2 * w) * 1024 + lane * 16;
  const unsigned voffH = (unsigned)(tile_m > 0 ? tile_m * 16 - 1 : 0) * 1024 + lane * 16;
  auto voffB = [&](int n) {
    const int r = (n & 3) * 8 + (lane >> 3), c = (lane & 7) ^ (sw_std(r) << 1);
    return (unsigned)(r * (3 * D_ * 2) + (n >> 2) * (D_ * 2) + c * 16);
  };
  const unsigned voffB0 = voffB(w);
  const unsigned voffX = (w < 4) ? voffB(8 + w) : voffH;
  const char* srcX = w < 4 ? srcB : srcA;
  const unsigned strX = w < 4 ? 32 * 3 * D_ * 2 : 128 * 1024, dstX = w < 4 ? 17 * 1024 + (8 + w) * 1024 : 16 * 1024;
  auto issue = [&](int st, unsigned slot) {
    const char* a = srcA + (size_t)st * (128 * 1024);
    const char* b = srcB + (size_t)st * (32 * 3 * D_ * 2);
    glds16(slot + 2 * w * 1024, voffA, a);
    glds16(slot + 2 * w * 1024 + 1024, voffA, a + 1024);
    glds16(slot + 17 * 1024 + w * 1024, voffB0, b);
    glds16(slot + dstX, voffX, srcX + (size_t)st * strX);
  };
  auto waitk = [&](int k) { wait_vm_k<4, G1_NS - 1>(k); };
  auto waitx = [&](int k) { wait_vm_k<4, G1_NS - 1, G1_PF>(k); };
  float gate = ((const float*)(ws + WS_G))[tile_m * 256 + (tid & 255)];
  asm volatile("" : "+v"(gate));
  const unsigned a_base = wm * 4096 + lane * 16;
  const unsigned ah_base = 16 * 1024 + lane * 16;
  const int trr = (lane >> 4) * 8 + ((lane & 15) >> 2), trp = lane & 3;
  auto tr_addr = [&](int jb) { return (unsigned)(17 * 1024 + trr * 128 + ((jb ^ sw_std(trr)) * 32) + trp * 8); };
  const unsigned b0 = tr_addr(2 * wn), b1 = tr_addr(2 * wn + 1);
  const unsigned bh = tr_addr(2 * wn + (wm & 1)) + (1 + (wm >> 1)) * 4096;

  f32x4 acc[4][6], acch = {0.f, 0.f, 0.f, 0.f};
#pragma unroll
  for (int m = 0; m < 4; ++m)
#pragma unroll
    for (int n = 0; n < 6; ++n) acc[m][n] = f32x4{0.f, 0.f, 0.f, 0.f};

#pragma unroll
  for (int s = 0; s < G1_NS; ++s) issue(s, s * G1_STAGE);
  waitk(G1_NS - 1);
  bar_raw();
  stp.at(1);
  G1Base qb[3];
#pragma unroll
  for (int i = 0; i < 3; ++i) {
    const unsigned o = i * 2 * G1_STAGE;
    qb[i] = G1Base{pin(a_base + o), pin(ah_base + o), pin(b0 + o), pin(b1 + o), pin(bh + o)};
  }
  G1Frags fx, fy;
  g1_read<0>(fx, qb[0]);
  auto step = [&](auto slc, auto tail, int t, G1Frags& fc, G1Frags& fn) {
    constexpr int SL = decltype(slc)::v, NX = (SL + 1) % G1_NS;
    if constexpr (decltype(tail)::v == 0) {
      waitk(G1_NS - 2);
      bar_lgkm();
      issue(t + G1_NS, SL * G1_STAGE);
    } else {
      if (t + G1_NS <= G1_T) waitk(G1_NS - 2);
      else waitx(G1_T - 2 - t);
      bar_lgkm();
      if (t + G1_NS < G1_T) issue(t + G1_NS, SL * G1_STAGE);
      else if (t + G1_NS == G1_T) warm<G1_PF>(pfb, pf0 + w * G1_PF, pfn, K1_DUMMY);
    }
    __builtin_amdgcn_sched_barrier(0);
    g1_read<(NX & 1) * G1_STAGE>(fn, qb[NX >> 1]);
    g1_mma(acc, acch, fc);
    interleave<25, 19>();
    __builtin_amdgcn_sched_barrier(0);
    asm volatile("" : "+v"(acch));
  };
  auto ten = [&](auto tail, int t) {
    step(IC<0>{}, tail, t, fx, fy);     step(IC<1>{}, tail, t + 1, fy, fx);
    step(IC<2>{}, tail, t + 2, fx, fy); step(IC<3>{}, tail, t + 3, fy, fx);
    step(IC<4>{}, tail, t + 4, fx, fy); step(IC<0>{}, tail, t + 5, fy, fx);
    step(IC<1>{}, tail, t + 6, fx, fy); step(IC<2>{}, tail, t + 7, fy, fx);
    step(IC<3>{}, tail, t + 8, fx, fy); step(IC<4>{}, tail, t + 9, fy, fx);
  };
  for (int t = 0; t < 20; t += 10) ten(IC<0>{}, t);
  ten(IC<1>{}, 20);
  step(IC<0>{}, IC<1>{}, 30, fx, fy);
  g1_mma(acc, acch, fy);
  bar_lgkm();
  stp.at(2);

  constexpr int PL = 272 * 16;
  const int tid2 = pin(threadIdx.x), ln = tid2 & 63;
  const int hd = tid2 >> 8, row = tid2 & 255, s = tile_m * 256 + row;
  {
    const unsigned wbase = wn * (12 * PL) + (ln >> 5) * PL + ((ln >> 4) & 1) * 8 + (ln & 15) * 16;
#pragma unroll
    for (int m = 0; m < 4; ++m)
#pragma unroll
      for (int n = 0; n < 6; ++n) {
        u32x2 o = {pack2(acc[m][n][0], acc[m][n][1]), pack2(acc[m][n][2], acc[m][n][3])};
        *(__attribute__((address_space(3))) u32x2*)(size_t)(wbase + n * 2 * PL + (16 + wm * 64 + m * 16) * 16) = o;
      }
    u32x2 o = {pack2(acch[0], acch[1]), pack2(acch[2], acch[3])};
    *(__attribute__((address_space(3))) u32x2*)(size_t)(wbase + (2 + wm) * 2 * PL) = o;
  }
  bar_lgkm();
  {
    const unsigned rbase = hd * (12 * PL) + (16 + row) * 16;
    auto ld = [&](int mat, int c, int back) {
      return *(const __attribute__((address_space(3))) u32x4*)(size_t)(rbase + (mat * 4 + c) * PL - back * 32);
    };
    u32x4 q[4];
#pragma unroll
    for (int c = 0; c < 4; ++c) q[c] = ld(0, c, 0);
    float lg[8];
#pragma unroll
    for (int wd = 0; wd < 8; ++wd) {
      float a = 0.f;
#pragma unroll
      for (int c = 0; c < 4; ++c) {
        u32x4 kk = ld(1, c, wd);
#pragma unroll
        for (int i = 0; i < 4; ++i)
          a = dot2(q[c][i], kk[i], a);
      }
      lg[wd] = (s - 2 * wd >= 0) ? a * 0.17677669529663687f : -1e9f;
    }
    float mx = lg[0];
#pragma unroll
    for (int wd = 1; wd < 8; ++wd) mx = fmaxf(mx, lg[wd]);
    float sum = 0.f;
#pragma unroll
    for (int wd = 0; wd < 8; ++wd) { lg[wd] = __expf(lg[wd] - mx); sum += lg[wd]; }
    const float sc = gate / sum;
    float o[32];
#pragma unroll
    for (int i = 0; i < 32; ++i) o[i] = 0.f;
#pragma unroll
    for (int wd = 0; wd < 8; ++wd) {
      const bool ok = (s - 2 * wd >= 0);
      const float p = lg[wd] * sc;
#pragma unroll
      for (int c = 0; c < 4; ++c) {
        u32x4 vv = ld(2, c, ok ? wd : 0);
#pragma unroll
        for (int i = 0; i < 4; ++i) {
          o[c * 8 + 2 * i] += p * bf_lo(vv[i]);
          o[c * 8 + 2 * i + 1] += p * bf_hi(vv[i]);
        }
      }
    }
    u32x4* dst = (u32x4*)(ws + WS_OT) + ((size_t)((tile_n * 2 + hd) * 128 + (s >> 4)) * 64 + (s & 15));
#pragma unroll
    for (int qq = 0; qq < 4; ++qq) {
      u32x4 r = {pack2(o[4 * qq], o[4 * qq + 1]), pack2(o[4 * qq + 2], o[4 * qq + 3]),
                 pack2(o[16 + 4 * qq], o[16 + 4 * qq + 1]), pack2(o[16 + 4 * qq + 2], o[16 + 4 * qq + 3])};
      dst[qq * 16] = r;
    }
  }
  wait_vm<0>();
  stp.at(3); stp.out(ws, 0);
}

struct G3Frags { bf16x8_t h[4], w[7]; };

DI float gelu_tanh(float u) {
  const float c0 = -2.0f * 0.7978845608028654f * 1.4426950408889634f;
  const float c1 = c0 * 0.044715f;
  const float e = __builtin_amdgcn_exp2f(u * __builtin_fmaf(u * u, c1, c0));
  return u * __builtin_amdgcn_rcpf(1.0f + e);
}

DI void gemm3_gelu(int tile_d, int tile_f, const char* __restrict__ w1, const char* __restrict__ b1, char* __restrict__ ws, const char* pfb, unsigned pf0, unsigned pfn) {
  const int tid = threadIdx.x, lane = tid & 63;
  const int w = __builtin_amdgcn_readfirstlane(tid >> 6);
  const int odd = tile_f & 1;
  Stamps stp; stp.at(0);
  if (w >= 4) {
    const int lw = w - 4;
    const char* srcA = ws + WS_HT;
    const char* srcB = w1 + (size_t)tile_f * 448;
    const unsigned voffA = (unsigned)(tile_d * 8 + 2 * lw) * 1024 + lane * 16;
    const int r = lw * 8 + (lane >> 3), c = (lane & 7) ^ (sw_std(r) << 1);
    const unsigned voffB = (unsigned)(r * (F_ * 2) + odd * 64 + c * 16);
    const int hg = (lw & 1) * 2 + ((lane >> 4) & 1), hr = hg * 8 + (lane >> 5) * 4 + ((lane >> 2) & 3);
    const int hc = (lane & 3) ^ ((hg & 1) << 1);
    const unsigned voffH = (unsigned)(hr * (F_ * 2) + (odd ? 0 : 384) + hc * 16);
    auto issue = [&](int st, unsigned slot) {
      const char* a = srcA + (size_t)st * (64 * 1024);
      const char* b = srcB + (size_t)st * (32 * F_ * 2);
      glds16(slot + 2 * lw * 1024, voffA, a);
      glds16(slot + 2 * lw * 1024 + 1024, voffA + 1024, a);
      glds16(slot + 8192 + lw * 1024, voffB, b);
      glds16(slot + 8192 + 4096 + lw * 1024, voffB + 128, b);
      glds16(slot + 8192 + 8192 + lw * 1024, voffB + 256, b);
      if (lw < 2) glds16(slot + 8192 + 12288 + lw * 1024, voffH, b);
    };
    auto waitk = [&](int k) {
      if (lw < 2) wait_vm_k<6, G3_NS - 1>(k);
      else wait_vm_k<5, G3_NS - 1>(k);
    };
    auto waitx = [&](int k) {
      if (lw < 2) wait_vm_k<6, G3_NS - 1, G3_PF>(k);
      else wait_vm_k<5, G3_NS - 1, G3_PF>(k);
    };
#pragma unroll
    for (int s = 0; s < G3_NS; ++s) issue(s, s * G3_STAGE);
    waitk(G3_NS - 1);
    bar_raw();
    unsigned cur = 0;
    for (int t = 0; t < G3_T - G3_NS; ++t) {
      waitk(G3_NS - 2);
      bar_raw();
      issue(t + G3_NS, cur);
      cur += G3_STAGE;
      if (cur == G3_NS * G3_STAGE) cur = 0;
    }
    waitk(G3_NS - 2);
    bar_raw();
    warm<G3_PF>(pfb, pf0 + lw * G3_PF, pfn, K1_DUMMY);
#pragma unroll
    for (int k = G3_NS - 3; k >= 0; --k) {
      waitx(k);
      bar_raw();
    }
    wait_vm<0>();
    return;
  }
  const int wd = w >> 1, wf = w & 1;
  const unsigned h_base = wd * 4096 + lane * 16;
  const int g = lane >> 4, qq = (lane & 15) >> 2, trp = lane & 3, trr = g * 8 + qq;
  unsigned wb[7];
  int fcol[7];
#pragma unroll
  for (int i = 0; i < 7; ++i) {
    const int lt = wf * 7 + i;
    const unsigned pa = 8192 + (lt >> 2) * 4096 + trr * 128 + (((lt & 3) ^ sw_std(trr)) * 32) + trp * 8;
    const unsigned ha = 8192 + 12288 + (g >> 1) * 1024 + (g & 1) * 256 + qq * 64 + (((lt & 1) ^ (g & 1)) * 32) + trp * 8;
    wb[i] = lt < 12 ? pa : ha;
    fcol[i] = tile_f * 224 + (odd ? (lt < 12 ? 32 + 16 * lt : 16 * (lt - 12)) : 16 * lt);
  }
  f32x4 acc[7][4];
#pragma unroll
  for (int i = 0; i < 7; ++i) {
    const u32x2 bias = *(const u32x2*)(b1 + (size_t)(fcol[i] + 4 * g) * 2);
    const unsigned b0 = bias[0], b1v = bias[1];
#pragma unroll
    for (int m = 0; m < 4; ++m) acc[i][m] = f32x4{bf_lo(b0), bf_hi(b0), bf_lo(b1v), bf_hi(b1v)};
  }
  unsigned hq[2], wq[2][7];
#pragma unroll
  for (int c = 0; c < 2; ++c) {
    hq[c] = pin(h_base + c * 3 * G3_STAGE);
#pragma unroll
    for (int i = 0; i < 7; ++i) wq[c][i] = pin(wb[i] + c * 3 * G3_STAGE);
  }
  auto rd = [&](G3Frags& f, auto slc) {
    constexpr int C = decltype(slc)::v / 3, SO = (decltype(slc)::v % 3) * G3_STAGE;
#pragma unroll
    for (int m = 0; m < 4; ++m) f.h[m] = lds_b128(hq[C] + (SO + m * 1024));
#pragma unroll
    for (int i = 0; i < 7; ++i) f.w[i] = lds_tr(wq[C][i] + SO, 512);
  };
  auto mm = [&](const G3Frags& f) {
#pragma unroll
    for (int i = 0; i < 7; ++i)
#pragma unroll
      for (int m = 0; m < 4; ++m) acc[i][m] = mfma(f.w[i], f.h[m], acc[i][m]);
  };
  G3Frags fx, fy;
  bar_raw();
  stp.at(1);
  rd(fx, IC<0>{});
  auto step = [&](auto nx, G3Frags& fc, G3Frags& fn) {
    bar_lgkm();
    __builtin_amdgcn_sched_barrier(0);
    rd(fn, nx);
    mm(fc);
    interleave<28, 18>();
    __builtin_amdgcn_sched_barrier(0);
  };
  for (int t = 0; t < 60; t += 6) {
    step(IC<1>{}, fx, fy); step(IC<2>{}, fy, fx); step(IC<3>{}, fx, fy);
    step(IC<4>{}, fy, fx); step(IC<5>{}, fx, fy); step(IC<0>{}, fy, fx);
  }
  step(IC<1>{}, fx, fy); step(IC<2>{}, fy, fx); step(IC<3>{}, fx, fy);
  mm(fy);
  stp.at(2);
  char* ut = ws + WS_UT;
  const int ln = pin(threadIdx.x) & 63;
#pragma unroll
  for (int m = 0; m < 4; ++m) {
    u32x2 pk[7];
#pragma unroll
    for (int i = 0; i < 7; ++i) {
      const float v0 = gelu_tanh(acc[i][m][0]), v1 = gelu_tanh(acc[i][m][1]);
      const float v2 = gelu_tanh(acc[i][m][2]), v3 = gelu_tanh(acc[i][m][3]);
      pk[i] = u32x2{pack2(v0, v1), pack2(v2, v3)};
    }
    const unsigned rbo = (unsigned)(tile_d * 8 + wd * 4 + m) * 1024 + ln * 16;
    if (wf == 0) {
#pragma unroll
      for (int p = 0; p < 3; ++p)
        *(u32x4*)(ut + (size_t)(fcol[2 * p] >> 5) * 65536 + rbo) = u32x4{pk[2 * p][0], pk[2 * p][1], pk[2 * p + 1][0], pk[2 * p + 1][1]};
      *(u32x2*)(ut + (size_t)(fcol[6] >> 5) * 65536 + rbo) = pk[6];
    } else {
      *(u32x2*)(ut + (size_t)(fcol[0] >> 5) * 65536 + rbo + 8) = pk[0];
#pragma unroll
      for (int p = 0; p < 3; ++p)
        *(u32x4*)(ut + (size_t)(fcol[2 * p + 1] >> 5) * 65536 + rbo) =
            u32x4{pk[2 * p + 1][0], pk[2 * p + 1][1], pk[2 * p + 2][0], pk[2 * p + 2][1]};
    }
  }
  stp.at(3); stp.out(ws, 0);
}

__global__ __launch_bounds__(512, 2) void k1_gemms(const char* __restrict__ wqkv, const char* __restrict__ w1,
                                                   const char* __restrict__ b1, char* __restrict__ ws,
                                                   const char* __restrict__ w2, const char* __restrict__ wo) {
  const int b = blockIdx.x, xcd = b & 7, j = b >> 3;
  const char* pfb = b < 224 ? w2 : wo;
  const unsigned pf0 = (b < 224 ? b : b - 224) * 64, pfn = b < 224 ? F_ * S_ * 2 / 1024 : D_ * D_ * 2 / 1024;
  if (xcd < 4) gemm1_attn((xcd & 1) * 4 + (j & 3), (xcd >> 1) * 8 + (j >> 2), wqkv, ws, pfb, pf0, pfn);
  else gemm3_gelu(j & 7, (xcd - 4) * 4 + (j >> 3), w1, b1, ws, pfb, pf0, pfn);
}

#define K2_NA 34
#define K2_POLL 12
#define K2_LOAD 14
constexpr int K2_STAGE = 32 * 1024, K2_NS = 5, K2_T1 = 56, K2_T = 72;
constexpr int K2_LDS = K2_STAGE * K2_NS;

struct K2Frags { bf16x8_t s[4], d[4]; };
struct K2Base { unsigned t[3][4], l[3]; };
template <int B, int E, class F>
DI void sfor(F&& f) {
  if constexpr (B < E) { f(IC<B>{}); sfor<B + 1, E>(f); }
}

template <bool RECV>
DI void k2_consumer(int tile_s, int tile_d, int w, const char* __restrict__ b2, char* __restrict__ ws, char* __restrict__ out) {
  const int lane = threadIdx.x & 63;
  Stamps stp; stp.at(0);
  const int ws_ = w >> 1, wd = w & 1;
  const int g = lane >> 4, trr = g * 4 + ((lane & 15) >> 2), trp = lane & 3;
  float gt[4], bb[4];
#pragma unroll
  for (int i = 0; i < 4; ++i) {
    const int s = tile_s * 128 + ws_ * 64 + i * 16 + (lane & 15);
    gt[i] = 1.0f - ((const float*)(ws + WS_G))[s];
    bb[i] = bf_lo(((const unsigned short*)b2)[s]);
  }
  f32x4 acc[4][4];
#pragma unroll
  for (int i = 0; i < 4; ++i)
#pragma unroll
    for (int n = 0; n < 4; ++n) acc[i][n] = f32x4{0.f, 0.f, 0.f, 0.f};
  K2Base q1, q2;
  auto mkbase = [&](K2Base& q, unsigned toff, unsigned loff) {
#pragma unroll
    for (int c = 0; c < 3; ++c) {
      q.l[c] = pin(lane * 16 + loff + c * 65536);
#pragma unroll
      for (int i = 0; i < 4; ++i) q.t[c][i] = pin(trr * 128 + ((i ^ sw_prm(trr)) * 32) + trp * 8 + toff + c * 65536);
    }
  };
  mkbase(q1, ws_ * 4096, 16384 + wd * 4096);
  auto rd = [&](K2Frags& f, auto part, auto slot, auto kkc) {
    constexpr int C = decltype(slot)::v >> 1, SO = (decltype(slot)::v & 1) * K2_STAGE + decltype(kkc)::v * 8192;
    if constexpr (decltype(part)::v == 1) {
#pragma unroll
      for (int i = 0; i < 4; ++i) {
        f.d[i] = lds_b128(q1.l[C] + (SO + i * 1024));
        f.s[i] = lds_tr(q1.t[C][i] + SO, 2048);
      }
    } else {
#pragma unroll
      for (int i = 0; i < 4; ++i) {
        f.s[i] = lds_b128(q2.l[C] + (SO + i * 1024));
        f.d[i] = lds_tr(q2.t[C][i] + SO, 2048);
      }
    }
  };
  auto mm = [&](const K2Frags& f) {
#pragma unroll
    for (int i = 0; i < 4; ++i)
#pragma unroll
      for (int n = 0; n < 4; ++n) acc[i][n] = mfma(f.d[n], f.s[i], acc[i][n]);
  };
#pragma unroll
  for (int i = 0; i < 4; ++i) asm volatile("" : "+v"(gt[i]), "+v"(bb[i]));
  K2Frags fx, fy;
  bar_raw();
  stp.at(1);
  rd(fx, IC<1>{}, IC<0>{}, IC<0>{});
  auto stage = [&](auto slot, auto pc, auto pn) {
    constexpr int SL = decltype(slot)::v % K2_NS;
    rd(fy, pc, IC<SL>{}, IC<1>{});
    mm(fx);
    interleave2<16, 12>();
    __builtin_amdgcn_sched_barrier(0);
    if constexpr (decltype(pn)::v != 0) {
      bar_lgkm();
      __builtin_amdgcn_sched_barrier(0);
      rd(fx, pn, IC<(SL + 1) % K2_NS>{}, IC<0>{});
    }
    mm(fy);
    interleave2<16, 12>();
    __builtin_amdgcn_sched_barrier(0);
  };
  constexpr int N1 = K2_T1 - K2_NA, ITS = (K2_NA - 1) / 5, ITR = (N1 - 1) / 5, TS = K2_NA - 5 * ITS, TR = N1 - 5 * ITR;
  for (int it = RECV ? ITR : ITS; it > 0; --it) sfor<0, 5>([&](auto i) { stage(i, IC<1>{}, IC<1>{}); });
  const int unit = (tile_s * 8 + tile_d) * 4 + w;
  const __amdgpu_buffer_rsrc_t rsP = __builtin_amdgcn_make_buffer_rsrc((void*)(ws + WS_P + (size_t)unit * 16384), 0, 16384, 0x00020000);
  unsigned* flag = (unsigned*)(ws + WS_FLAG + (size_t)unit * 128);
  if constexpr (!RECV) {
    sfor<0, TS - 1>([&](auto i) { stage(i, IC<1>{}, IC<1>{}); });
    stage(IC<TS - 1>{}, IC<1>{}, IC<0>{});
    stp.at(2);
    const int ln = pin(threadIdx.x) & 63;
#pragma unroll
    for (int i = 0; i < 4; ++i)
#pragma unroll
      for (int n = 0; n < 4; ++n) {
        const f32x4 v = acc[i][n] * gt[i];
        __builtin_amdgcn_raw_buffer_store_b128(__builtin_bit_cast(u32x4, v), rsP, ln * 16 + (i * 4 + n) * 1024, 0, 16);
      }
    wait_vm<0>();
    if (ln == 0) __hip_atomic_store(flag, 1u, __ATOMIC_RELAXED, __HIP_MEMORY_SCOPE_AGENT);
    stp.at(3); stp.out(ws, 1);
  } else {
  sfor<0, TR - 1>([&](auto i) { stage(i, IC<1>{}, IC<1>{}); });
  mkbase(q2, 16384 + wd * 4096, ws_ * 4096);
  stage(IC<TR - 1>{}, IC<1>{}, IC<2>{});
#pragma unroll
  for (int i = 0; i < 4; ++i)
#pragma unroll
    for (int n = 0; n < 4; ++n)
#pragma unroll
      for (int e = 0; e < 4; ++e) acc[i][n][e] = (acc[i][n][e] + bb[i]) * gt[i];
  __builtin_amdgcn_sched_barrier(0);
  for (int it = 0; it < 2; ++it) sfor<TR, TR + 5>([&](auto i) { stage(i, IC<2>{}, IC<2>{}); });
  sfor<TR + 10, TR + K2_POLL>([&](auto i) { stage(i, IC<2>{}, IC<2>{}); });
  unsigned fl = __hip_atomic_load(flag, __ATOMIC_RELAXED, __HIP_MEMORY_SCOPE_AGENT);
  __builtin_amdgcn_sched_barrier(0);
  sfor<TR + K2_POLL, TR + K2_LOAD>([&](auto i) { stage(i, IC<2>{}, IC<(decltype(i)::v == TR + 15 ? 0 : 2)>{}); });
#pragma unroll
  for (int i = 0; i < 4; ++i) asm volatile("" : "+v"(acc[i][0]), "+v"(acc[i][1]), "+v"(acc[i][2]), "+v"(acc[i][3]));
  for (int spin = 0; __builtin_amdgcn_readfirstlane(fl) == 0 && spin < (1 << 20); ++spin) {
    __builtin_amdgcn_s_sleep(1);
    fl = __hip_atomic_load(flag, __ATOMIC_RELAXED, __HIP_MEMORY_SCOPE_AGENT);
  }
  asm volatile("" ::: "memory");
  const int ln = pin(threadIdx.x) & 63;
  u32x4 part[4][4];
#pragma unroll
  for (int i = 0; i < 4; ++i)
#pragma unroll
    for (int n = 0; n < 4; ++n) part[i][n] = __builtin_amdgcn_raw_buffer_load_b128(rsP, ln * 16, (i * 4 + n) * 1024, 16);
  __builtin_amdgcn_sched_barrier(0);
  sfor<TR + K2_LOAD, TR + 16>([&](auto i) { stage(i, IC<2>{}, IC<(decltype(i)::v == TR + 15 ? 0 : 2)>{}); });
  stp.at(2);
  char* op = out + ((size_t)(tile_s * 128 + ws_ * 64 + (ln & 15)) * D_ + tile_d * 128 + wd * 64 + (ln >> 4) * 4) * 2;
#pragma unroll
  for (int i = 0; i < 4; ++i)
#pragma unroll
    for (int n = 0; n < 4; ++n) {
      const f32x4 v = acc[i][n] + __builtin_bit_cast(f32x4, part[i][n]);
      *(u32x2*)(op + (i * 16 * D_ + n * 16) * 2) = u32x2{pack2(v[0], v[1]), pack2(v[2], v[3])};
    }
  stp.at(3); stp.out(ws, 1);
  }
}

__global__ __launch_bounds__(512, 2) void k2_out(const char* __restrict__ wo, const char* __restrict__ w2,
                                                 const char* __restrict__ b2, char* __restrict__ ws,
                                                 char* __restrict__ out) {
  const int b = blockIdx.x, xcd = b & 7, j = b >> 3;
  const int recv = xcd & 1, quad = xcd >> 1;
  const int tile_s = (quad & 1) * 8 + (j & 7), tile_d = (quad >> 1) * 4 + (j >> 3);
  const int tid = threadIdx.x, lane = tid & 63;
  const int w = __builtin_amdgcn_readfirstlane(tid >> 6);
  if (w >= 4) {
    const int lw = w - 4;
    const int st0 = recv ? K2_NA : 0, nst = recv ? K2_T - K2_NA : K2_NA;
    const int r = lw * 8 + (lane >> 3), c = (lane & 7) ^ (sw_prm(r) << 1);
    const unsigned voffW2 = (unsigned)(r * (S_ * 2) + tile_s * 256 + c * 16);
    const unsigned voffU = (unsigned)(tile_d * 8 + 2 * lw) * 1024 + lane * 16;
    const unsigned voffO = (unsigned)(tile_s * 8 + 2 * lw) * 1024 + lane * 16;
    const unsigned voffWo = (unsigned)(r * (D_ * 2) + tile_d * 256 + c * 16);
    auto issue = [&](int st, unsigned slot) {
      if (st < K2_T1) {
        const char* a = w2 + (size_t)st * (64 * S_ * 2);
        const char* u = ws + WS_UT + (size_t)st * (2 * 65536);
#pragma unroll
        for (int kk = 0; kk < 2; ++kk) {
          glds16(slot + kk * 8192 + lw * 1024, voffW2 + kk * (32 * S_ * 2), a);
          glds16(slot + kk * 8192 + 4096 + lw * 1024, voffW2 + kk * (32 * S_ * 2) + 128, a);
          glds16(slot + 16384 + kk * 8192 + 2 * lw * 1024, voffU + kk * 65536, u);
          glds16(slot + 16384 + kk * 8192 + 2 * lw * 1024 + 1024, voffU + kk * 65536 + 1024, u);
        }
      } else {
        const char* o = ws + WS_OT + (size_t)(st - K2_T1) * (2 * 131072);
        const char* a = wo + (size_t)(st - K2_T1) * (64 * D_ * 2);
#pragma unroll
        for (int kk = 0; kk < 2; ++kk) {
          glds16(slot + kk * 8192 + 2 * lw * 1024, voffO + kk * 131072, o);
          glds16(slot + kk * 8192 + 2 * lw * 1024 + 1024, voffO + kk * 131072 + 1024, o);
          glds16(slot + 16384 + kk * 8192 + lw * 1024, voffWo + kk * (32 * D_ * 2), a);
          glds16(slot + 16384 + kk * 8192 + 4096 + lw * 1024, voffWo + kk * (32 * D_ * 2) + 128, a);
        }
      }
    };
#pragma unroll
    for (int s = 0; s < K2_NS; ++s) issue(st0 + s, s * K2_STAGE);
    wait_vm<8 * (K2_NS - 1)>();
    bar_raw();
    unsigned cur = 0;
    for (int t = 0; t < nst - 1; ++t) {
      wait_vm_k<8, K2_NS - 2>(min(K2_NS - 2, nst - 2 - t));
      bar_raw();
      if (t + K2_NS < nst) issue(st0 + t + K2_NS, cur);
      cur += K2_STAGE;
      if (cur == K2_NS * K2_STAGE) cur = 0;
    }
    return;
  }
  if (recv) k2_consumer<true>(tile_s, tile_d, w, b2, ws, out);
  else k2_consumer<false>(tile_s, tile_d, w, b2, ws, out);
}

extern "C" void kernel_launch(void* const* d_in, const int* in_sizes, int n_in, void* d_out, int out_size, void* d_ws,
                              size_t ws_size, hipStream_t stream) {
  const char* x = (const char*)d_in[0];
  const char* wqkv = (const char*)d_in[1];
  const char* wo = (const char*)d_in[2];
  const char* w1 = (const char*)d_in[4];
  const char* b1 = (const char*)d_in[5];
  const char* w2 = (const char*)d_in[6];
  const char* b2 = (const char*)d_in[7];
  const char* gw = (const char*)d_in[8];
  char* ws = (char*)d_ws;
  (void)hipFuncSetAttribute((const void*)k1_gemms, hipFuncAttributeMaxDynamicSharedMemorySize, K1_LDS);
  (void)hipFuncSetAttribute((const void*)k2_out, hipFuncAttributeMaxDynamicSharedMemorySize, K2_LDS);
  k0_prep<<<256, 512, 0, stream>>>(x, gw, ws, w1);
  k1_gemms<<<256, 512, K1_LDS, stream>>>(wqkv, w1, b1, ws, w2, wo);
  k2_out<<<256, 512, K2_LDS, stream>>>(wo, w2, b2, ws, (char*)d_out);
}
```

```cpp
#include <hip/hip_runtime.h>
#include <hip/hip_bf16.h>

typedef __attribute__((ext_vector_type(2))) __bf16 bf16x2_t;
typedef __attribute__((ext_vector_type(8))) __bf16 bf16x8_t;
typedef __attribute__((ext_vector_type(4))) short s16x4;
typedef __attribute__((ext_vector_type(8))) short s16x8;
typedef __attribute__((ext_vector_type(4))) float f32x4;
typedef __attribute__((ext_vector_type(2))) float f32x2;
typedef __attribute__((ext_vector_type(4))) unsigned u32x4;
typedef __attribute__((ext_vector_type(2))) unsigned u32x2;
typedef __attribute__((address_space(3))) char* lds_ptr;

#define DI __device__ __forceinline__

constexpr int S_ = 2048, D_ = 1024, F_ = 3584;

constexpr size_t WS_XT = 0;
constexpr size_t WS_HT = WS_XT + (size_t)S_ * D_ * 2;
constexpr size_t WS_OT = WS_HT + (size_t)S_ * D_ * 2;
constexpr size_t WS_UT = WS_OT + (size_t)S_ * D_ * 2;
constexpr size_t WS_G = WS_UT + (size_t)D_ * F_ * 2;
constexpr size_t WS_P = WS_G + 8192;
constexpr size_t WS_FLAG = WS_P + (size_t)S_ * D_ * 4;

struct Stamps {
  DI void at(int) {}
  DI void out(const char*, int) {}
};

DI unsigned pack2(float a, float b) {
  f32x2 f = {a, b};
  return __builtin_bit_cast(unsigned, __builtin_convertvector(f, bf16x2_t));
}
DI float bf_lo(unsigned u) { return __builtin_bit_cast(float, u << 16); }
DI float bf_hi(unsigned u) { return __builtin_bit_cast(float, u & 0xffff0000u); }

DI float dot2(unsigned a, unsigned b, float c) {
  return __builtin_amdgcn_fdot2_f32_bf16(__builtin_bit_cast(bf16x2_t, a), __builtin_bit_cast(bf16x2_t, b), c, false);
}

DI void glds16(unsigned lds_dst, unsigned voff, const void* sbase) {
  asm volatile("s_mov_b32 m0, %0\n\ts_nop 0\n\tglobal_load_lds_dwordx4 %1, %2" ::"s"(lds_dst), "v"(voff), "s"(sbase)
               : "memory");
}
template <int N>
DI void wait_vm() {
  asm volatile("s_waitcnt vmcnt(%0)" ::"i"(N) : "memory");
}
DI void bar_raw() { asm volatile("s_barrier" ::: "memory"); }
DI void bar_lgkm() {
  __builtin_amdgcn_s_waitcnt(0xC07F);
  asm volatile("s_barrier" ::: "memory");
}

DI bf16x8_t lds_b128(unsigned addr) {
  return *(const __attribute__((address_space(3))) bf16x8_t*)(size_t)addr;
}
DI bf16x8_t lds_tr(unsigned addr, unsigned off2) {
  s16x4 lo = __builtin_amdgcn_ds_read_tr16_b64_v4i16((__attribute__((address_space(3))) s16x4*)(size_t)addr);
  s16x4 hi = __builtin_amdgcn_ds_read_tr16_b64_v4i16((__attribute__((address_space(3))) s16x4*)(size_t)(addr + off2));
  s16x8 r = __builtin_shufflevector(lo, hi, 0, 1, 2, 3, 4, 5, 6, 7);
  return __builtin_bit_cast(bf16x8_t, r);
}
DI f32x4 mfma(bf16x8_t a, bf16x8_t b, f32x4 c) { return __builtin_amdgcn_mfma_f32_16x16x32_bf16(a, b, c, 0, 0, 0); }

#define AUX0 0
#define AUX1 16
#define AUX1H 0
#define AUX2 16
typedef __amdgpu_buffer_rsrc_t rsrc_t;
DI rsrc_t mkrs(const void* p, unsigned bytes) { return __builtin_amdgcn_make_buffer_rsrc((void*)p, 0, bytes, 0x00020000); }
template <int AUX> DI void st16(rsrc_t rs, unsigned off, u32x4 v) { __builtin_amdgcn_raw_buffer_store_b128(v, rs, off, 0, AUX); }
template <int AUX> DI void st8(rsrc_t rs, unsigned off, u32x2 v) { __builtin_amdgcn_raw_buffer_store_b64(v, rs, off, 0, AUX); }

template <int V> struct IC { static constexpr int v = V; };
DI unsigned pin(unsigned v) { asm volatile("" : "+v"(v)); return v; }

template <int N, int KMAX, int X = 0>
DI void wait_vm_k(int k) {
  if (k >= KMAX) wait_vm<N * KMAX + X>();
  else if constexpr (KMAX > 0) wait_vm_k<N, KMAX - 1, X>(k);
}

template <int NP>
DI void warm(const char* base, unsigned piece0, unsigned npieces, unsigned lds_dummy) {
  const unsigned lane16 = (threadIdx.x & 63) * 16;
#pragma unroll
  for (int i = 0; i < NP; ++i) {
    const unsigned pc = min(piece0 + i, npieces - 1);
    glds16(lds_dummy, pc * 1024 + lane16, base);
  }
}

template <int CTRL>
DI float dpp_add(float v) {
  return v + __builtin_bit_cast(float, __builtin_amdgcn_update_dpp(0, __builtin_bit_cast(int, v), CTRL, 0xf, 0xf, true));
}
DI float rdlane(float v, int l) { return __builtin_bit_cast(float, __builtin_amdgcn_readlane(__builtin_bit_cast(int, v), l)); }
DI float wave_sum(float v) {
  v = dpp_add<0xB1>(v);
  v = dpp_add<0x4E>(v);
  v = dpp_add<0x141>(v);
  v = dpp_add<0x140>(v);
  return (rdlane(v, 0) + rdlane(v, 16)) + (rdlane(v, 32) + rdlane(v, 48));
}
DI void gload16(u32x4& d, unsigned voff, const void* sbase) {
  asm volatile("global_load_dwordx4 %0, %1, %2" : "=v"(d) : "v"(voff), "s"(sbase) : "memory");
}

constexpr int K0_PITCH = 2048 + 16;
__global__ __launch_bounds__(512) void k0_prep(const char* __restrict__ x, const char* __restrict__ gw, char* __restrict__ ws,
                                               const char* __restrict__ w1) {
  __shared__ __attribute__((aligned(16))) char lds[2 * 8 * K0_PITCH + 1024];
  const unsigned lx = (unsigned)(size_t)(lds_ptr)lds, lh = lx + 8 * K0_PITCH;
  const int t = threadIdx.x, lane = t & 63;
  const int wv = __builtin_amdgcn_readfirstlane(t >> 6);
  const int s0 = blockIdx.x * 8;
  Stamps stp; stp.at(0);
  u32x4 raw[2], gwv[2];
  gload16(raw[0], (s0 + wv) * 2048 + lane * 16, x);
  gload16(raw[1], (s0 + wv) * 2048 + 1024 + lane * 16, x);
  gload16(gwv[0], lane * 16, gw);
  gload16(gwv[1], 1024 + lane * 16, gw);
  warm<7>(w1, (blockIdx.x * 8 + wv) * 7, S_ * F_ * 2 / 1024, lh + 8 * K0_PITCH);
  asm volatile("s_waitcnt vmcnt(7)" : "+v"(raw[0]), "+v"(raw[1]), "+v"(gwv[0]), "+v"(gwv[1]));
  stp.at(1);
  typedef __attribute__((address_space(3))) u32x4* l128;
  *(l128)(size_t)(lx + wv * K0_PITCH + lane * 16) = raw[0];
  *(l128)(size_t)(lx + wv * K0_PITCH + 1024 + lane * 16) = raw[1];
  float v[16], sm = 0.f, gd = 0.f;
#pragma unroll
  for (int i = 0; i < 8; ++i) {
    const unsigned r = raw[i >> 2][i & 3], g = gwv[i >> 2][i & 3];
    v[2 * i] = bf_lo(r); v[2 * i + 1] = bf_hi(r);
    sm += v[2 * i] + v[2 * i + 1];
    gd += v[2 * i] * bf_lo(g) + v[2 * i + 1] * bf_hi(g);
  }
  const float mean = wave_sum(sm) * (1.0f / D_);
  gd = wave_sum(gd);
  float vr = 0.f;
#pragma unroll
  for (int i = 0; i < 16; ++i) { v[i] -= mean; vr += v[i] * v[i]; }
  const float rs = rsqrtf(wave_sum(vr) * (1.0f / D_) + 1e-5f);
#pragma unroll
  for (int h = 0; h < 2; ++h) {
    u32x4 o;
#pragma unroll
    for (int i = 0; i < 4; ++i) o[i] = pack2(v[8 * h + 2 * i] * rs, v[8 * h + 2 * i + 1] * rs);
    *(l128)(size_t)(lh + wv * K0_PITCH + h * 1024 + lane * 16) = o;
  }
  if (lane == 0) ((float*)(ws + WS_G))[s0 + wv] = 1.0f / (1.0f + __expf(-gd));
  if (t < 2) *(unsigned*)(ws + WS_FLAG + (size_t)(blockIdx.x * 2 + t) * 128) = 0u;
  bar_lgkm();
  const rsrc_t rw = mkrs(ws, (unsigned)WS_G);
#pragma unroll
  for (int h = 0; h < 2; ++h) {
    const int ch = h * 64 + (t >> 3), r = t & 7;
    const u32x4 o = *(l128)(size_t)(lx + r * K0_PITCH + ch * 16);
    st16<AUX0>(rw, (unsigned)WS_XT + (((ch >> 2) * 128 + (s0 >> 4)) * 64 + (ch & 3) * 16 + (s0 & 15) + r) * 16, o);
    const int d0 = wv * 128 + h * 64;
    const bf16x8_t c = lds_tr(lh + ((lane & 15) >> 2) * K0_PITCH + (d0 + (lane >> 4) * 16 + (lane & 3) * 4) * 2, 4 * K0_PITCH);
    const int d = d0 + lane;
    st16<AUX0>(rw, (unsigned)WS_HT + (((s0 >> 5) * 64 + (d >> 4)) * 64 + ((s0 & 31) >> 3) * 16 + (d & 15)) * 16, __builtin_bit_cast(u32x4, c));
  }
  stp.at(2);
  wait_vm<0>();
  stp.at(3); stp.out(ws, 2);
}

DI int sw_std(int r) { return ((r >> 1) & 1) | (((r >> 3) & 1) << 1); }
DI int sw_prm(int r) { return (r >> 1) & 3; }

constexpr int G1_STAGE = 29 * 1024, G1_NS = 5, G1_T = 32;
constexpr int G3_STAGE = 22 * 1024, G3_NS = 6, G3_T = 64;
constexpr int K1_DUMMY = G1_STAGE * G1_NS;
constexpr int K1_LDS = K1_DUMMY + 1024;
static_assert(G3_STAGE * G3_NS <= K1_DUMMY, "");
constexpr int G1_PF = 8, G3_PF = 16;

struct G1Frags { bf16x8_t a[4], ah, b[6], bh; };

struct G1Base { unsigned a, ah, b0, b1, bh; };
template <int SO>
DI void g1_read(G1Frags& f, const G1Base& q) {
#pragma unroll
  for (int p = 0; p < 3; ++p) {
    f.b[2 * p] = lds_tr(q.b0 + (SO + p * 4096), 512);
    f.b[2 * p + 1] = lds_tr(q.b1 + (SO + p * 4096), 512);
  }
#pragma unroll
  for (int m = 0; m < 4; ++m) f.a[m] = lds_b128(q.a + (SO + m * 1024));
  f.bh = lds_tr(q.bh + SO, 512);
  f.ah = lds_b128(q.ah + SO);
}
DI void g1_mma(f32x4 (&acc)[4][6], f32x4& acch, const G1Frags& f) {
  acch = mfma(f.bh, f.ah, acch);
#pragma unroll
  for (int m = 0; m < 4; ++m)
#pragma unroll
    for (int n = 0; n < 6; ++n)
      acc[m][n] = n < 4 ? mfma(f.b[n], f.a[m], acc[m][n]) : mfma(f.a[m], f.b[n], acc[m][n]);
}
template <int NMFMA, int NDS>
DI void interleave() {
#pragma unroll
  for (int i = 0; i < NMFMA; ++i) {
    __builtin_amdgcn_sched_group_barrier(0x008, 1, 0);
    if (i < NDS) __builtin_amdgcn_sched_group_barrier(0x100, 1, 0);
    __builtin_amdgcn_sched_group_barrier(0x002, 1, 0);
  }
}

template <int NMFMA, int NDS>
DI void interleave2() {
#pragma unroll
  for (int i = 0; i < NMFMA; ++i) {
    __builtin_amdgcn_sched_group_barrier(0x008, 1, 0);
    if (2 * i < NDS) __builtin_amdgcn_sched_group_barrier(0x100, 2, 0);
    __builtin_amdgcn_sched_group_barrier(0x002, 1, 0);
  }
}

DI void gemm1_attn(int tile_m, int tile_n, const char* __restrict__ wqkv, char* __restrict__ ws, const char* pfb, unsigned pf0, unsigned pfn) {
  const int tid = threadIdx.x, lane = tid & 63;
  const int w = __builtin_amdgcn_readfirstlane(tid >> 6);
  const int wm = w >> 1, wn = w & 1;
  Stamps stp; stp.at(0);
  const char* srcA = ws + WS_XT;
  const char* srcB = wqkv + (size_t)tile_n * 128;
  const unsigned voffA = (unsigned)(tile_m * 16 + 2 * w) * 1024 + lane * 16;
  const unsigned voffH = (unsigned)(tile_m > 0 ? tile_m * 16 - 1 : 0) * 1024 + lane * 16;
  auto voffB = [&](int n) {
    const int r = (n & 3) * 8 + (lane >> 3), c = (lane & 7) ^ (sw_std(r) << 1);
    return (unsigned)(r * (3 * D_ * 2) + (n >> 2) * (D_ * 2) + c * 16);
  };
  const unsigned voffB0 = voffB(w);
  const unsigned voffX = (w < 4) ? voffB(8 + w) : voffH;
  const char* srcX = w < 4 ? srcB : srcA;
  const unsigned strX = w < 4 ? 32 * 3 * D_ * 2 : 128 * 1024, dstX = w < 4 ? 17 * 1024 + (8 + w) * 1024 : 16 * 1024;
  auto issue = [&](int st, unsigned slot) {
    const char* a = srcA + (size_t)st * (128 * 1024);
    const char* b = srcB + (size_t)st * (32 * 3 * D_ * 2);
    glds16(slot + 2 * w * 1024, voffA, a);
    glds16(slot + 2 * w * 1024 + 1024, voffA, a + 1024);
    glds16(slot + 17 * 1024 + w * 1024, voffB0, b);
    glds16(slot + dstX, voffX, srcX + (size_t)st * strX);
  };
  auto waitk = [&](int k) { wait_vm_k<4, G1_NS - 1>(k); };
  auto waitx = [&](int k) { wait_vm_k<4, G1_NS - 1, G1_PF>(k); };
  float gate = ((const float*)(ws + WS_G))[tile_m * 256 + wm * 64 + lane];
  asm volatile("" : "+v"(gate));
  const unsigned a_base = wm * 4096 + lane * 16;
  const unsigned ah_base = 16 * 1024 + lane * 16;
  const int trr = (lane >> 4) * 8 + ((lane & 15) >> 2), trp = lane & 3;
  auto tr_addr = [&](int jb) { return (unsigned)(17 * 1024 + trr * 128 + ((jb ^ sw_std(trr)) * 32) + trp * 8); };
  const unsigned b0 = tr_addr(2 * wn), b1 = tr_addr(2 * wn + 1);
  const unsigned bh = tr_addr(2 * wn + (wm & 1)) + (1 + (wm >> 1)) * 4096;

  f32x4 acc[4][6], acch = {0.f, 0.f, 0.f, 0.f};
#pragma unroll
  for (int m = 0; m < 4; ++m)
#pragma unroll
    for (int n = 0; n < 6; ++n) acc[m][n] = f32x4{0.f, 0.f, 0.f, 0.f};

#pragma unroll
  for (int s = 0; s < G1_NS; ++s) issue(s, s * G1_STAGE);
  waitk(G1_NS - 1);
  bar_raw();
  stp.at(1);
  G1Base qb[3];
#pragma unroll
  for (int i = 0; i < 3; ++i) {
    const unsigned o = i * 2 * G1_STAGE;
    qb[i] = G1Base{pin(a_base + o), pin(ah_base + o), pin(b0 + o), pin(b1 + o), pin(bh + o)};
  }
  G1Frags fx, fy;
  g1_read<0>(fx, qb[0]);
  auto step = [&](auto slc, auto tail, int t, G1Frags& fc, G1Frags& fn) {
    constexpr int SL = decltype(slc)::v, NX = (SL + 1) % G1_NS;
    if constexpr (decltype(tail)::v == 0) {
      waitk(G1_NS - 2);
      bar_lgkm();
      issue(t + G1_NS, SL * G1_STAGE);
    } else {
      if (t + G1_NS <= G1_T) waitk(G1_NS - 2);
      else waitx(G1_T - 2 - t);
      bar_lgkm();
      if (t + G1_NS < G1_T) issue(t + G1_NS, SL * G1_STAGE);
      else if (t + G1_NS == G1_T) warm<G1_PF>(pfb, pf0 + w * G1_PF, pfn, K1_DUMMY);
    }
    __builtin_amdgcn_sched_barrier(0);
    g1_read<(NX & 1) * G1_STAGE>(fn, qb[NX >> 1]);
    g1_mma(acc, acch, fc);
    interleave<25, 19>();
    __builtin_amdgcn_sched_barrier(0);
    asm volatile("" : "+v"(acch));
  };
  auto ten = [&](auto tail, int t) {
    step(IC<0>{}, tail, t, fx, fy);     step(IC<1>{}, tail, t + 1, fy, fx);
    step(IC<2>{}, tail, t + 2, fx, fy); step(IC<3>{}, tail, t + 3, fy, fx);
    step(IC<4>{}, tail, t + 4, fx, fy); step(IC<0>{}, tail, t + 5, fy, fx);
    step(IC<1>{}, tail, t + 6, fx, fy); step(IC<2>{}, tail, t + 7, fy, fx);
    step(IC<3>{}, tail, t + 8, fx, fy); step(IC<4>{}, tail, t + 9, fy, fx);
  };
  for (int t = 0; t < 20; t += 10) ten(IC<0>{}, t);
  ten(IC<1>{}, 20);
  step(IC<0>{}, IC<1>{}, 30, fx, fy);
  g1_mma(acc, acch, fy);
  bar_lgkm();
  stp.at(2);

  const int ln = pin(threadIdx.x) & 63, qi = ln & 15, g = ln >> 4;
  typedef __attribute__((address_space(3))) u32x2* l64;
  u32x2 pk[4][6];
#pragma unroll
  for (int m = 0; m < 4; ++m)
#pragma unroll
    for (int n = 0; n < 6; ++n) pk[m][n] = u32x2{pack2(acc[m][n][0], acc[m][n][1]), pack2(acc[m][n][2], acc[m][n][3])};
  const unsigned xb = wn * 8192;
  if (wm < 3) {
#pragma unroll
    for (int n = 2; n < 6; ++n) *(l64)(size_t)(xb + (wm + 1) * 2048 + (n - 2) * 512 + ln * 8) = pk[3][n];
  }
  *(l64)(size_t)(xb + wm * 512 + (wm < 2 ? ln * 8 : qi * 32 + g * 8)) = u32x2{pack2(acch[0], acch[1]), pack2(acch[2], acch[3])};
  bar_lgkm();
  u32x2 pv[4];
  {
    const unsigned ra = xb + wm * 2048 + ln * 8;
    pv[0] = *(l64)(size_t)ra;
    pv[1] = *(l64)(size_t)(ra + 512);
    if (wm == 0) {
      pv[2] = __builtin_bit_cast(u32x2, __builtin_amdgcn_ds_read_tr16_b64_v4i16((__attribute__((address_space(3))) s16x4*)(size_t)(ra + 1024)));
      pv[3] = __builtin_bit_cast(u32x2, __builtin_amdgcn_ds_read_tr16_b64_v4i16((__attribute__((address_space(3))) s16x4*)(size_t)(ra + 1536)));
    } else {
      pv[2] = *(l64)(size_t)(ra + 1024);
      pv[3] = *(l64)(size_t)(ra + 1536);
    }
  }
  auto frag = [](u32x2 a, u32x2 b) { return __builtin_bit_cast(bf16x8_t, u32x4{a[0], a[1], b[0], b[1]}); };
  auto fbits = [](unsigned u) { return __builtin_bit_cast(float, u); };
  auto ubits = [](float f) { return __builtin_bit_cast(unsigned, f); };
  const bool odd = qi & 1;
  const bool cur0 = 4 * g + (qi & 1) <= qi, cur1 = 4 * g + 2 + (qi & 1) <= qi;
  const rsrc_t ro = mkrs(ws + WS_OT, S_ * D_ * 2);
  const unsigned dst = ((tile_n * 2 + wn) * 128 + tile_m * 16 + wm * 4) * 1024 + ln * 16;
  const f32x4 z4 = {0.f, 0.f, 0.f, 0.f};
#pragma unroll
  for (int mi = 0; mi < 4; ++mi) {
    const int m = (mi + 1) & 3;
    const u32x2 k0 = m ? pk[m ? m - 1 : 0][2] : pv[0], k1 = m ? pk[m ? m - 1 : 0][3] : pv[1];
    const u32x2 v0 = m ? pk[m ? m - 1 : 0][4] : pv[2], v1 = m ? pk[m ? m - 1 : 0][5] : pv[3];
    const bf16x8_t q = frag(pk[m][0], pk[m][1]);
    const f32x4 sp = mfma(frag(k0, k1), q, z4), sc = mfma(frag(pk[m][2], pk[m][3]), q, z4);
    const float cs = 0.17677669529663687f * 1.4426950408889634f;
    float l0 = (cur0 ? (odd ? sc[1] : sc[0]) : (odd ? sp[1] : sp[0])) * cs;
    float l1 = (cur1 ? (odd ? sc[3] : sc[2]) : (odd ? sp[3] : sp[2])) * cs;
    if (m == 0 && tile_m == 0 && wm == 0) {
      l0 = cur0 ? l0 : -1e9f;
      l1 = cur1 ? l1 : -1e9f;
    }
    float mx = fmaxf(l0, l1);
    u32x2 t = __builtin_amdgcn_permlane16_swap(ubits(mx), ubits(mx), false, false);
    mx = fmaxf(fbits(t[0]), fbits(t[1]));
    t = __builtin_amdgcn_permlane32_swap(ubits(mx), ubits(mx), false, false);
    mx = fmaxf(fbits(t[0]), fbits(t[1]));
    const unsigned pp = pack2(__builtin_amdgcn_exp2f(l0 - mx), __builtin_amdgcn_exp2f(l1 - mx));
    float sum = bf_lo(pp) + bf_hi(pp);
    t = __builtin_amdgcn_permlane16_swap(ubits(sum), ubits(sum), false, false);
    sum = fbits(t[0]) + fbits(t[1]);
    t = __builtin_amdgcn_permlane32_swap(ubits(sum), ubits(sum), false, false);
    sum = fbits(t[0]) + fbits(t[1]);
    const unsigned t0 = odd ? pp << 16 : pp & 0xffffu, t1 = odd ? pp & 0xffff0000u : pp >> 16;
    const bf16x8_t pf = frag(u32x2{cur0 ? 0u : t0, cur1 ? 0u : t1}, u32x2{cur0 ? t0 : 0u, cur1 ? t1 : 0u});
    const f32x4 o0 = mfma(frag(v0, pk[m][4]), pf, z4), o1 = mfma(frag(v1, pk[m][5]), pf, z4);
    const float gs = fbits(__builtin_amdgcn_ds_bpermute((m * 16 + qi) * 4, ubits(gate))) * __builtin_amdgcn_rcpf(sum);
    st16<AUX1>(ro, dst + m * 1024,
               u32x4{pack2(o0[0] * gs, o0[1] * gs), pack2(o0[2] * gs, o0[3] * gs), pack2(o1[0] * gs, o1[1] * gs), pack2(o1[2] * gs, o1[3] * gs)});
  }
  wait_vm<0>();
  stp.at(3); stp.out(ws, 0);
}

struct G3Frags { bf16x8_t h[4], w[7]; };

DI unsigned gelu_tanh2(float u0, float u1) {
  const float c0 = -2.0f * 0.7978845608028654f * 1.4426950408889634f;
  const float c1 = c0 * 0.044715f;
  const f32x2 u = {u0, u1};
  const f32x2 a = u * __builtin_elementwise_fma(u * u, f32x2{c1, c1}, f32x2{c0, c0});
  const f32x2 d = f32x2{__builtin_amdgcn_exp2f(a[0]), __builtin_amdgcn_exp2f(a[1])} + f32x2{1.0f, 1.0f};
  const f32x2 r = u * f32x2{__builtin_amdgcn_rcpf(d[0]), __builtin_amdgcn_rcpf(d[1])};
  return __builtin_bit_cast(unsigned, __builtin_convertvector(r, bf16x2_t));
}

DI void gemm3_gelu(int tile_d, int tile_f, const char* __restrict__ w1, const char* __restrict__ b1, char* __restrict__ ws, const char* pfb, unsigned pf0, unsigned pfn) {
  const int tid = threadIdx.x, lane = tid & 63;
  const int w = __builtin_amdgcn_readfirstlane(tid >> 6);
  const int odd = tile_f & 1;
  Stamps stp; stp.at(0);
  if (w >= 4) {
    const int lw = w - 4;
    const char* srcA = ws + WS_HT;
    const char* srcB = w1 + (size_t)tile_f * 448;
    const unsigned voffA = (unsigned)(tile_d * 8 + 2 * lw) * 1024 + lane * 16;
    const int r = lw * 8 + (lane >> 3), c = (lane & 7) ^ (sw_std(r) << 1);
    const unsigned voffB = (unsigned)(r * (F_ * 2) + odd * 64 + c * 16);
    const int hg = (lw & 1) * 2 + ((lane >> 4) & 1), hr = hg * 8 + (lane >> 5) * 4 + ((lane >> 2) & 3);
    const int hc = (lane & 3) ^ ((hg & 1) << 1);
    const unsigned voffH = (unsigned)(hr * (F_ * 2) + (odd ? 0 : 384) + hc * 16);
    auto issue = [&](int st, unsigned slot) {
      const char* a = srcA + (size_t)st * (64 * 1024);
      const char* b = srcB + (size_t)st * (32 * F_ * 2);
      glds16(slot + 2 * lw * 1024, voffA, a);
      glds16(slot + 2 * lw * 1024 + 1024, voffA + 1024, a);
      glds16(slot + 8192 + lw * 1024, voffB, b);
      glds16(slot + 8192 + 4096 + lw * 1024, voffB + 128, b);
      glds16(slot + 8192 + 8192 + lw * 1024, voffB + 256, b);
      if (lw < 2) glds16(slot + 8192 + 12288 + lw * 1024, voffH, b);
    };
    auto waitk = [&](int k) {
      if (lw < 2) wait_vm_k<6, G3_NS - 1>(k);
      else wait_vm_k<5, G3_NS - 1>(k);
    };
    auto waitx = [&](int k) {
      if (lw < 2) wait_vm_k<6, G3_NS - 1, G3_PF>(k);
      else wait_vm_k<5, G3_NS - 1, G3_PF>(k);
    };
#pragma unroll
    for (int s = 0; s < G3_NS; ++s) issue(s, s * G3_STAGE);
    waitk(G3_NS - 1);
    bar_raw();
    unsigned cur = 0;
    for (int t = 0; t < G3_T - G3_NS; ++t) {
      waitk(G3_NS - 2);
      bar_raw();
      issue(t + G3_NS, cur);
      cur += G3_STAGE;
      if (cur == G3_NS * G3_STAGE) cur = 0;
    }
    waitk(G3_NS - 2);
    bar_raw();
    warm<G3_PF>(pfb, pf0 + lw * G3_PF, pfn, K1_DUMMY);
#pragma unroll
    for (int k = G3_NS - 3; k >= 0; --k) {
      waitx(k);
      bar_raw();
    }
    wait_vm<0>();
    return;
  }
  const int wd = w >> 1, wf = w & 1;
  const unsigned h_base = wd * 4096 + lane * 16;
  const int g = lane >> 4, qq = (lane & 15) >> 2, trp = lane & 3, trr = g * 8 + qq;
  unsigned wb[7];
  int fcol[7];
#pragma unroll
  for (int i = 0; i < 7; ++i) {
    const int lt = wf * 7 + i;
    const unsigned pa = 8192 + (lt >> 2) * 4096 + trr * 128 + (((lt & 3) ^ sw_std(trr)) * 32) + trp * 8;
    const unsigned ha = 8192 + 12288 + (g >> 1) * 1024 + (g & 1) * 256 + qq * 64 + (((lt & 1) ^ (g & 1)) * 32) + trp * 8;
    wb[i] = lt < 12 ? pa : ha;
    fcol[i] = tile_f * 224 + (odd ? (lt < 12 ? 32 + 16 * lt : 16 * (lt - 12)) : 16 * lt);
  }
  f32x4 acc[7][4];
#pragma unroll
  for (int i = 0; i < 7; ++i) {
    const u32x2 bias = *(const u32x2*)(b1 + (size_t)(fcol[i] + 4 * g) * 2);
    const unsigned b0 = bias[0], b1v = bias[1];
#pragma unroll
    for (int m = 0; m < 4; ++m) acc[i][m] = f32x4{bf_lo(b0), bf_hi(b0), bf_lo(b1v), bf_hi(b1v)};
  }
  unsigned hq[2], wq[2][7];
#pragma unroll
  for (int c = 0; c < 2; ++c) {
    hq[c] = pin(h_base + c * 3 * G3_STAGE);
#pragma unroll
    for (int i = 0; i < 7; ++i) wq[c][i] = pin(wb[i] + c * 3 * G3_STAGE);
  }
  auto rd = [&](G3Frags& f, auto slc) {
    constexpr int C = decltype(slc)::v / 3, SO = (decltype(slc)::v % 3) * G3_STAGE;
#pragma unroll
    for (int m = 0; m < 4; ++m) f.h[m] = lds_b128(hq[C] + (SO + m * 1024));
#pragma unroll
    for (int i = 0; i < 7; ++i) f.w[i] = lds_tr(wq[C][i] + SO, 512);
  };
  auto mm = [&](const G3Frags& f) {
#pragma unroll
    for (int i = 0; i < 7; ++i)
#pragma unroll
      for (int m = 0; m < 4; ++m) acc[i][m] = mfma(f.w[i], f.h[m], acc[i][m]);
  };
  G3Frags fx, fy;
  bar_raw();
  stp.at(1);
  rd(fx, IC<0>{});
  auto step = [&](auto nx, G3Frags& fc, G3Frags& fn) {
    bar_lgkm();
    __builtin_amdgcn_sched_barrier(0);
    rd(fn, nx);
    mm(fc);
    interleave<28, 18>();
    __builtin_amdgcn_sched_barrier(0);
  };
  for (int t = 0; t < 60; t += 6) {
    step(IC<1>{}, fx, fy); step(IC<2>{}, fy, fx); step(IC<3>{}, fx, fy);
    step(IC<4>{}, fy, fx); step(IC<5>{}, fx, fy); step(IC<0>{}, fy, fx);
  }
  step(IC<1>{}, fx, fy); step(IC<2>{}, fy, fx); step(IC<3>{}, fx, fy);
  mm(fy);
  stp.at(2);
  const rsrc_t ru = mkrs(ws + WS_UT, D_ * F_ * 2);
  const int ln = pin(threadIdx.x) & 63;
#pragma unroll
  for (int m = 0; m < 4; ++m) {
    u32x2 pk[7];
#pragma unroll
    for (int i = 0; i < 7; ++i) {
      pk[i] = u32x2{gelu_tanh2(acc[i][m][0], acc[i][m][1]), gelu_tanh2(acc[i][m][2], acc[i][m][3])};
    }
    const unsigned rbo = (unsigned)(tile_d * 8 + wd * 4 + m) * 1024 + ln * 16;
    if (wf == 0) {
#pragma unroll
      for (int p = 0; p < 3; ++p)
        st16<AUX1>(ru, (fcol[2 * p] >> 5) * 65536 + rbo, u32x4{pk[2 * p][0], pk[2 * p][1], pk[2 * p + 1][0], pk[2 * p + 1][1]});
      st8<AUX1H>(ru, (fcol[6] >> 5) * 65536 + rbo, pk[6]);
    } else {
      st8<AUX1H>(ru, (fcol[0] >> 5) * 65536 + rbo + 8, pk[0]);
#pragma unroll
      for (int p = 0; p < 3; ++p)
        st16<AUX1>(ru, (fcol[2 * p + 1] >> 5) * 65536 + rbo,
                   u32x4{pk[2 * p + 1][0], pk[2 * p + 1][1], pk[2 * p + 2][0], pk[2 * p + 2][1]});
    }
  }
  stp.at(3); stp.out(ws, 0);
}

__global__ __launch_bounds__(512, 2) void k1_gemms(const char* __restrict__ wqkv, const char* __restrict__ w1,
                                                   const char* __restrict__ b1, char* __restrict__ ws,
                                                   const char* __restrict__ w2, const char* __restrict__ wo) {
  const int b = blockIdx.x, xcd = b & 7, j = b >> 3;
  const char* pfb = b < 224 ? w2 : wo;
  const unsigned pf0 = (b < 224 ? b : b - 224) * 64, pfn = b < 224 ? F_ * S_ * 2 / 1024 : D_ * D_ * 2 / 1024;
  if (xcd < 4) gemm1_attn((xcd & 1) * 4 + (j & 3), (xcd >> 1) * 8 + (j >> 2), wqkv, ws, pfb, pf0, pfn);
  else gemm3_gelu(j & 7, (xcd - 4) * 4 + (j >> 3), w1, b1, ws, pfb, pf0, pfn);
}

#define K2_NA 34
#define K2_POLL 12
#define K2_LOAD 14
constexpr int K2_STAGE = 32 * 1024, K2_NS = 5, K2_T1 = 56, K2_T = 72;
constexpr int K2_LDS = K2_STAGE * K2_NS;

struct K2Frags { bf16x8_t s[4], d[4]; };
struct K2Base { unsigned t[3][4], l[3]; };
template <int B, int E, class F>
DI void sfor(F&& f) {
  if constexpr (B < E) { f(IC<B>{}); sfor<B + 1, E>(f); }
}

template <bool RECV>
DI void k2_consumer(int tile_s, int tile_d, int w, const char* __restrict__ b2, char* __restrict__ ws, char* __restrict__ out) {
  const int lane = threadIdx.x & 63;
  Stamps stp; stp.at(0);
  const int ws_ = w >> 1, wd = w & 1;
  const int g = lane >> 4, trr = g * 4 + ((lane & 15) >> 2), trp = lane & 3;
  float gt[4], bb[4];
#pragma unroll
  for (int i = 0; i < 4; ++i) {
    const int s = tile_s * 128 + ws_ * 64 + i * 16 + (lane & 15);
    gt[i] = 1.0f - ((const float*)(ws + WS_G))[s];
    bb[i] = bf_lo(((const unsigned short*)b2)[s]);
  }
  f32x4 acc[4][4];
#pragma unroll
  for (int i = 0; i < 4; ++i)
#pragma unroll
    for (int n = 0; n < 4; ++n) acc[i][n] = f32x4{0.f, 0.f, 0.f, 0.f};
  K2Base q1, q2;
  auto mkbase = [&](K2Base& q, unsigned toff, unsigned loff) {
#pragma unroll
    for (int c = 0; c < 3; ++c) {
      q.l[c] = pin(lane * 16 + loff + c * 65536);
#pragma unroll
      for (int i = 0; i < 4; ++i) q.t[c][i] = pin(trr * 128 + ((i ^ sw_prm(trr)) * 32) + trp * 8 + toff + c * 65536);
    }
  };
  mkbase(q1, ws_ * 4096, 16384 + wd * 4096);
  auto rd = [&](K2Frags& f, auto part, auto slot, auto kkc) {
    constexpr int C = decltype(slot)::v >> 1, SO = (decltype(slot)::v & 1) * K2_STAGE + decltype(kkc)::v * 8192;
    if constexpr (decltype(part)::v == 1) {
#pragma unroll
      for (int i = 0; i < 4; ++i) {
        f.d[i] = lds_b128(q1.l[C] + (SO + i * 1024));
        f.s[i] = lds_tr(q1.t[C][i] + SO, 2048);
      }
    } else {
#pragma unroll
      for (int i = 0; i < 4; ++i) {
        f.s[i] = lds_b128(q2.l[C] + (SO + i * 1024));
        f.d[i] = lds_tr(q2.t[C][i] + SO, 2048);
      }
    }
  };
  auto mm = [&](const K2Frags& f) {
#pragma unroll
    for (int i = 0; i < 4; ++i)
#pragma unroll
      for (int n = 0; n < 4; ++n) acc[i][n] = mfma(f.d[n], f.s[i], acc[i][n]);
  };
#pragma unroll
  for (int i = 0; i < 4; ++i) asm volatile("" : "+v"(gt[i]), "+v"(bb[i]));
  K2Frags fx, fy;
  bar_raw();
  stp.at(1);
  rd(fx, IC<1>{}, IC<0>{}, IC<0>{});
  auto stage = [&](auto slot, auto pc, auto pn) {
    constexpr int SL = decltype(slot)::v % K2_NS;
    rd(fy, pc, IC<SL>{}, IC<1>{});
    mm(fx);
    interleave2<16, 12>();
    __builtin_amdgcn_sched_barrier(0);
    if constexpr (decltype(pn)::v != 0) {
      bar_lgkm();
      __builtin_amdgcn_sched_barrier(0);
      rd(fx, pn, IC<(SL + 1) % K2_NS>{}, IC<0>{});
    }
    mm(fy);
    interleave2<16, 12>();
    __builtin_amdgcn_sched_barrier(0);
  };
  constexpr int N1 = K2_T1 - K2_NA, ITS = (K2_NA - 1) / 5, ITR = (N1 - 1) / 5, TS = K2_NA - 5 * ITS, TR = N1 - 5 * ITR;
  for (int it = RECV ? ITR : ITS; it > 0; --it) sfor<0, 5>([&](auto i) { stage(i, IC<1>{}, IC<1>{}); });
  const int unit = (tile_s * 8 + tile_d) * 4 + w;
  const __amdgpu_buffer_rsrc_t rsP = __builtin_amdgcn_make_buffer_rsrc((void*)(ws + WS_P + (size_t)unit * 16384), 0, 16384, 0x00020000);
  unsigned* flag = (unsigned*)(ws + WS_FLAG + (size_t)unit * 128);
  if constexpr (!RECV) {
    sfor<0, TS - 1>([&](auto i) { stage(i, IC<1>{}, IC<1>{}); });
    stage(IC<TS - 1>{}, IC<1>{}, IC<0>{});
    stp.at(2);
    const int ln = pin(threadIdx.x) & 63;
#pragma unroll
    for (int i = 0; i < 4; ++i)
#pragma unroll
      for (int n = 0; n < 4; ++n) {
        const f32x4 v = acc[i][n] * gt[i];
        __builtin_amdgcn_raw_buffer_store_b128(__builtin_bit_cast(u32x4, v), rsP, ln * 16 + (i * 4 + n) * 1024, 0, 16);
      }
    wait_vm<0>();
    if (ln == 0) __hip_atomic_store(flag, 1u, __ATOMIC_RELAXED, __HIP_MEMORY_SCOPE_AGENT);
    stp.at(3); stp.out(ws, 1);
  } else {
  sfor<0, TR - 1>([&](auto i) { stage(i, IC<1>{}, IC<1>{}); });
  mkbase(q2, 16384 + wd * 4096, ws_ * 4096);
  stage(IC<TR - 1>{}, IC<1>{}, IC<2>{});
#pragma unroll
  for (int i = 0; i < 4; ++i)
#pragma unroll
    for (int n = 0; n < 4; ++n)
#pragma unroll
      for (int e = 0; e < 4; ++e) acc[i][n][e] = (acc[i][n][e] + bb[i]) * gt[i];
  __builtin_amdgcn_sched_barrier(0);
  for (int it = 0; it < 2; ++it) sfor<TR, TR + 5>([&](auto i) { stage(i, IC<2>{}, IC<2>{}); });
  sfor<TR + 10, TR + K2_POLL>([&](auto i) { stage(i, IC<2>{}, IC<2>{}); });
  unsigned fl = __hip_atomic_load(flag, __ATOMIC_RELAXED, __HIP_MEMORY_SCOPE_AGENT);
  __builtin_amdgcn_sched_barrier(0);
  sfor<TR + K2_POLL, TR + K2_LOAD>([&](auto i) { stage(i, IC<2>{}, IC<(decltype(i)::v == TR + 15 ? 0 : 2)>{}); });
#pragma unroll
  for (int i = 0; i < 4; ++i) asm volatile("" : "+v"(acc[i][0]), "+v"(acc[i][1]), "+v"(acc[i][2]), "+v"(acc[i][3]));
  for (int spin = 0; __builtin_amdgcn_readfirstlane(fl) == 0 && spin < (1 << 20); ++spin) {
    __builtin_amdgcn_s_sleep(1);
    fl = __hip_atomic_load(flag, __ATOMIC_RELAXED, __HIP_MEMORY_SCOPE_AGENT);
  }
  asm volatile("" ::: "memory");
  const int ln = pin(threadIdx.x) & 63;
  u32x4 part[4][4];
#pragma unroll
  for (int i = 0; i < 4; ++i)
#pragma unroll
    for (int n = 0; n < 4; ++n) part[i][n] = __builtin_amdgcn_raw_buffer_load_b128(rsP, ln * 16, (i * 4 + n) * 1024, 16);
  __builtin_amdgcn_sched_barrier(0);
  sfor<TR + K2_LOAD, TR + 16>([&](auto i) { stage(i, IC<2>{}, IC<(decltype(i)::v == TR + 15 ? 0 : 2)>{}); });
  stp.at(2);
  const rsrc_t rout = mkrs(out, S_ * D_ * 2);
  const unsigned oo = ((tile_s * 128 + ws_ * 64 + (ln & 15)) * D_ + tile_d * 128 + wd * 64 + ((ln >> 4) & 1) * 16 + (ln >> 5) * 8) * 2;
#pragma unroll
  for (int i = 0; i < 4; ++i)
#pragma unroll
    for (int n = 0; n < 4; n += 2) {
      const f32x4 v = acc[i][n] + __builtin_bit_cast(f32x4, part[i][n]);
      const f32x4 u = acc[i][n + 1] + __builtin_bit_cast(f32x4, part[i][n + 1]);
      const u32x2 lo = __builtin_amdgcn_permlane16_swap(pack2(v[0], v[1]), pack2(u[0], u[1]), false, false);
      const u32x2 hi = __builtin_amdgcn_permlane16_swap(pack2(v[2], v[3]), pack2(u[2], u[3]), false, false);
      st16<AUX2>(rout, oo + (i * 16 * D_ + n * 16) * 2, u32x4{lo[0], hi[0], lo[1], hi[1]});
    }
  stp.at(3); stp.out(ws, 1);
  }
}

__global__ __launch_bounds__(512, 2) void k2_out(const char* __restrict__ wo, const char* __restrict__ w2,
                                                 const char* __restrict__ b2, char* __restrict__ ws,
                                                 char* __restrict__ out) {
  const int b = blockIdx.x, xcd = b & 7, j = b >> 3;
  const int recv = xcd & 1, quad = xcd >> 1;
  const int tile_s = (quad & 1) * 8 + (j & 7), tile_d = (quad >> 1) * 4 + (j >> 3);
  const int tid = threadIdx.x, lane = tid & 63;
  const int w = __builtin_amdgcn_readfirstlane(tid >> 6);
  if (w >= 4) {
    const int lw = w - 4;
    const int st0 = recv ? K2_NA : 0, nst = recv ? K2_T - K2_NA : K2_NA;
    const int r = lw * 8 + (lane >> 3), c = (lane & 7) ^ (sw_prm(r) << 1);
    const unsigned voffW2 = (unsigned)(r * (S_ * 2) + tile_s * 256 + c * 16);
    const unsigned voffU = (unsigned)(tile_d * 8 + 2 * lw) * 1024 + lane * 16;
    const unsigned voffO = (unsigned)(tile_s * 8 + 2 * lw) * 1024 + lane * 16;
    const unsigned voffWo = (unsigned)(r * (D_ * 2) + tile_d * 256 + c * 16);
    auto issue = [&](int st, unsigned slot) {
      if (st < K2_T1) {
        const char* a = w2 + (size_t)st * (64 * S_ * 2);
        const char* u = ws + WS_UT + (size_t)st * (2 * 65536);
#pragma unroll
        for (int kk = 0; kk < 2; ++kk) {
          glds16(slot + kk * 8192 + lw * 1024, voffW2 + kk * (32 * S_ * 2), a);
          glds16(slot + kk * 8192 + 4096 + lw * 1024, voffW2 + kk * (32 * S_ * 2) + 128, a);
          glds16(slot + 16384 + kk * 8192 + 2 * lw * 1024, voffU + kk * 65536, u);
          glds16(slot + 16384 + kk * 8192 + 2 * lw * 1024 + 1024, voffU + kk * 65536 + 1024, u);
        }
      } else {
        const char* o = ws + WS_OT + (size_t)(st - K2_T1) * (2 * 131072);
        const char* a = wo + (size_t)(st - K2_T1) * (64 * D_ * 2);
#pragma unroll
        for (int kk = 0; kk < 2; ++kk) {
          glds16(slot + kk * 8192 + 2 * lw * 1024, voffO + kk * 131072, o);
          glds16(slot + kk * 8192 + 2 * lw * 1024 + 1024, voffO + kk * 131072 + 1024, o);
          glds16(slot + 16384 + kk * 8192 + lw * 1024, voffWo + kk * (32 * D_ * 2), a);
          glds16(slot + 16384 + kk * 8192 + 4096 + lw * 1024, voffWo + kk * (32 * D_ * 2) + 128, a);
        }
      }
    };
#pragma unroll
    for (int s = 0; s < K2_NS; ++s) issue(st0 + s, s * K2_STAGE);
    wait_vm<8 * (K2_NS - 1)>();
    bar_raw();
    unsigned cur = 0;
    for (int t = 0; t < nst - 1; ++t) {
      wait_vm_k<8, K2_NS - 2>(min(K2_NS - 2, nst - 2 - t));
      bar_raw();
      if (t + K2_NS < nst) issue(st0 + t + K2_NS, cur);
      cur += K2_STAGE;
      if (cur == K2_NS * K2_STAGE) cur = 0;
    }
    return;
  }
  if (recv) k2_consumer<true>(tile_s, tile_d, w, b2, ws, out);
  else k2_consumer<false>(tile_s, tile_d, w, b2, ws, out);
}

extern "C" void kernel_launch(void* const* d_in, const int* in_sizes, int n_in, void* d_out, int out_size, void* d_ws,
                              size_t ws_size, hipStream_t stream) {
  const char* x = (const char*)d_in[0];
  const char* wqkv = (const char*)d_in[1];
  const char* wo = (const char*)d_in[2];
  const char* w1 = (const char*)d_in[4];
  const char* b1 = (const char*)d_in[5];
  const char* w2 = (const char*)d_in[6];
  const char* b2 = (const char*)d_in[7];
  const char* gw = (const char*)d_in[8];
  char* ws = (char*)d_ws;
  (void)hipFuncSetAttribute((const void*)k1_gemms, hipFuncAttributeMaxDynamicSharedMemorySize, K1_LDS);
  (void)hipFuncSetAttribute((const void*)k2_out, hipFuncAttributeMaxDynamicSharedMemorySize, K2_LDS);
  k0_prep<<<256, 512, 0, stream>>>(x, gw, ws, w1);
  k1_gemms<<<256, 512, K1_LDS, stream>>>(wqkv, w1, b1, ws, w2, wo);
  k2_out<<<256, 512, K2_LDS, stream>>>(wo, w2, b2, ws, (char*)d_out);
}
```

```cpp
#include <hip/hip_runtime.h>
#include <hip/hip_bf16.h>

typedef __attribute__((ext_vector_type(2))) __bf16 bf16x2_t;
typedef __attribute__((ext_vector_type(8))) __bf16 bf16x8_t;
typedef __attribute__((ext_vector_type(4))) short s16x4;
typedef __attribute__((ext_vector_type(8))) short s16x8;
typedef __attribute__((ext_vector_type(4))) float f32x4;
typedef __attribute__((ext_vector_type(2))) float f32x2;
typedef __attribute__((ext_vector_type(4))) unsigned u32x4;
typedef __attribute__((ext_vector_type(2))) unsigned u32x2;
typedef __attribute__((address_space(3))) char* lds_ptr;

#define DI __device__ __forceinline__

constexpr int S_ = 2048, D_ = 1024, F_ = 3584;

constexpr size_t WS_XT = 0;
constexpr size_t WS_HT = WS_XT + (size_t)S_ * D_ * 2;
constexpr size_t WS_OT = WS_HT + (size_t)S_ * D_ * 2;
constexpr size_t WS_UT = WS_OT + (size_t)S_ * D_ * 2;
constexpr size_t WS_G = WS_UT + (size_t)D_ * F_ * 2;
constexpr size_t WS_P = WS_G + 8192;
constexpr size_t WS_FLAG = WS_P + (size_t)S_ * D_ * 2;

struct Stamps {
  DI void at(int) {}
  DI void out(const char*, int) {}
};

DI unsigned pack2(float a, float b) {
  f32x2 f = {a, b};
  return __builtin_bit_cast(unsigned, __builtin_convertvector(f, bf16x2_t));
}
DI float bf_lo(unsigned u) { return __builtin_bit_cast(float, u << 16); }
DI float bf_hi(unsigned u) { return __builtin_bit_cast(float, u & 0xffff0000u); }

DI float dot2(unsigned a, unsigned b, float c) {
  return __builtin_amdgcn_fdot2_f32_bf16(__builtin_bit_cast(bf16x2_t, a), __builtin_bit_cast(bf16x2_t, b), c, false);
}

DI void glds16(unsigned lds_dst, unsigned voff, const void* sbase) {
  asm volatile("s_mov_b32 m0, %0\n\ts_nop 0\n\tglobal_load_lds_dwordx4 %1, %2" ::"s"(lds_dst), "v"(voff), "s"(sbase)
               : "memory");
}
template <int N>
DI void wait_vm() {
  asm volatile("s_waitcnt vmcnt(%0)" ::"i"(N) : "memory");
}
DI void bar_raw() { asm volatile("s_barrier" ::: "memory"); }
DI void bar_lgkm() {
  __builtin_amdgcn_s_waitcnt(0xC07F);
  asm volatile("s_barrier" ::: "memory");
}

DI bf16x8_t lds_b128(unsigned addr) {
  return *(const __attribute__((address_space(3))) bf16x8_t*)(size_t)addr;
}
DI bf16x8_t lds_tr(unsigned addr, unsigned off2) {
  s16x4 lo = __builtin_amdgcn_ds_read_tr16_b64_v4i16((__attribute__((address_space(3))) s16x4*)(size_t)addr);
  s16x4 hi = __builtin_amdgcn_ds_read_tr16_b64_v4i16((__attribute__((address_space(3))) s16x4*)(size_t)(addr + off2));
  s16x8 r = __builtin_shufflevector(lo, hi, 0, 1, 2, 3, 4, 5, 6, 7);
  return __builtin_bit_cast(bf16x8_t, r);
}
DI f32x4 mfma(bf16x8_t a, bf16x8_t b, f32x4 c) { return __builtin_amdgcn_mfma_f32_16x16x32_bf16(a, b, c, 0, 0, 0); }

#define AUX0 0
#define AUX1 16
#define AUX1H 0
#define AUX2 16
typedef __amdgpu_buffer_rsrc_t rsrc_t;
DI rsrc_t mkrs(const void* p, unsigned bytes) { return __builtin_amdgcn_make_buffer_rsrc((void*)p, 0, bytes, 0x00020000); }
template <int AUX> DI void st16(rsrc_t rs, unsigned off, u32x4 v) { __builtin_amdgcn_raw_buffer_store_b128(v, rs, off, 0, AUX); }
template <int AUX> DI void st8(rsrc_t rs, unsigned off, u32x2 v) { __builtin_amdgcn_raw_buffer_store_b64(v, rs, off, 0, AUX); }

template <int V> struct IC { static constexpr int v = V; };
DI unsigned pin(unsigned v) { asm volatile("" : "+v"(v)); return v; }

template <int N, int KMAX, int X = 0>
DI void wait_vm_k(int k) {
  if (k >= KMAX) wait_vm<N * KMAX + X>();
  else if constexpr (KMAX > 0) wait_vm_k<N, KMAX - 1, X>(k);
}

template <int NP>
DI void warm(const char* base, unsigned piece0, unsigned npieces, unsigned lds_dummy) {
  const unsigned lane16 = (threadIdx.x & 63) * 16;
#pragma unroll
  for (int i = 0; i < NP; ++i) {
    const unsigned pc = min(piece0 + i, npieces - 1);
    glds16(lds_dummy, pc * 1024 + lane16, base);
  }
}

template <int CTRL>
DI float dpp_add(float v) {
  return v + __builtin_bit_cast(float, __builtin_amdgcn_update_dpp(0, __builtin_bit_cast(int, v), CTRL, 0xf, 0xf, true));
}
DI float rdlane(float v, int l) { return __builtin_bit_cast(float, __builtin_amdgcn_readlane(__builtin_bit_cast(int, v), l)); }
DI float wave_sum(float v) {
  v = dpp_add<0xB1>(v);
  v = dpp_add<0x4E>(v);
  v = dpp_add<0x141>(v);
  v = dpp_add<0x140>(v);
  return (rdlane(v, 0) + rdlane(v, 16)) + (rdlane(v, 32) + rdlane(v, 48));
}
DI void gload16(u32x4& d, unsigned voff, const void* sbase) {
  asm volatile("global_load_dwordx4 %0, %1, %2" : "=v"(d) : "v"(voff), "s"(sbase) : "memory");
}

constexpr int K0_PITCH = 2048 + 16;
__global__ __launch_bounds__(512) void k0_prep(const char* __restrict__ x, const char* __restrict__ gw, char* __restrict__ ws,
                                               const char* __restrict__ w1) {
  __shared__ __attribute__((aligned(16))) char lds[2 * 8 * K0_PITCH + 1024];
  const unsigned lx = (unsigned)(size_t)(lds_ptr)lds, lh = lx + 8 * K0_PITCH;
  const int t = threadIdx.x, lane = t & 63;
  const int wv = __builtin_amdgcn_readfirstlane(t >> 6);
  const int s0 = blockIdx.x * 8;
  Stamps stp; stp.at(0);
  u32x4 raw[2], gwv[2];
  gload16(raw[0], (s0 + wv) * 2048 + lane * 16, x);
  gload16(raw[1], (s0 + wv) * 2048 + 1024 + lane * 16, x);
  gload16(gwv[0], lane * 16, gw);
  gload16(gwv[1], 1024 + lane * 16, gw);
  warm<7>(w1, (blockIdx.x * 8 + wv) * 7, S_ * F_ * 2 / 1024, lh + 8 * K0_PITCH);
  asm volatile("s_waitcnt vmcnt(7)" : "+v"(raw[0]), "+v"(raw[1]), "+v"(gwv[0]), "+v"(gwv[1]));
  stp.at(1);
  typedef __attribute__((address_space(3))) u32x4* l128;
  *(l128)(size_t)(lx + wv * K0_PITCH + lane * 16) = raw[0];
  *(l128)(size_t)(lx + wv * K0_PITCH + 1024 + lane * 16) = raw[1];
  float v[16], sm = 0.f, gd = 0.f;
#pragma unroll
  for (int i = 0; i < 8; ++i) {
    const unsigned r = raw[i >> 2][i & 3], g = gwv[i >> 2][i & 3];
    v[2 * i] = bf_lo(r); v[2 * i + 1] = bf_hi(r);
    sm += v[2 * i] + v[2 * i + 1];
    gd += v[2 * i] * bf_lo(g) + v[2 * i + 1] * bf_hi(g);
  }
  const float mean = wave_sum(sm) * (1.0f / D_);
  gd = wave_sum(gd);
  float vr = 0.f;
#pragma unroll
  for (int i = 0; i < 16; ++i) { v[i] -= mean; vr += v[i] * v[i]; }
  const float rs = rsqrtf(wave_sum(vr) * (1.0f / D_) + 1e-5f);
#pragma unroll
  for (int h = 0; h < 2; ++h) {
    u32x4 o;
#pragma unroll
    for (int i = 0; i < 4; ++i) o[i] = pack2(v[8 * h + 2 * i] * rs, v[8 * h + 2 * i + 1] * rs);
    *(l128)(size_t)(lh + wv * K0_PITCH + h * 1024 + lane * 16) = o;
  }
  if (lane == 0) ((float*)(ws + WS_G))[s0 + wv] = 1.0f / (1.0f + __expf(-gd));
  if (t < 2) *(unsigned*)(ws + WS_FLAG + (size_t)(blockIdx.x * 2 + t) * 128) = 0u;
  bar_lgkm();
  const rsrc_t rw = mkrs(ws, (unsigned)WS_G);
#pragma unroll
  for (int h = 0; h < 2; ++h) {
    const int ch = h * 64 + (t >> 3), r = t & 7;
    const u32x4 o = *(l128)(size_t)(lx + r * K0_PITCH + ch * 16);
    st16<AUX0>(rw, (unsigned)WS_XT + (((ch >> 2) * 128 + (s0 >> 4)) * 64 + (ch & 3) * 16 + (s0 & 15) + r) * 16, o);
    const int d0 = wv * 128 + h * 64;
    const bf16x8_t c = lds_tr(lh + ((lane & 15) >> 2) * K0_PITCH + (d0 + (lane >> 4) * 16 + (lane & 3) * 4) * 2, 4 * K0_PITCH);
    const int d = d0 + lane;
    st16<AUX0>(rw, (unsigned)WS_HT + (((s0 >> 5) * 64 + (d >> 4)) * 64 + ((s0 & 31) >> 3) * 16 + (d & 15)) * 16, __builtin_bit_cast(u32x4, c));
  }
  stp.at(2);
  wait_vm<0>();
  stp.at(3); stp.out(ws, 2);
}

DI int sw_std(int r) { return ((r >> 1) & 1) | (((r >> 3) & 1) << 1); }
DI int sw_prm(int r) { return (r >> 1) & 3; }

constexpr int G1_STAGE = 29 * 1024, G1_NS = 5, G1_T = 32;
constexpr int G3_STAGE = 22 * 1024, G3_NS = 6, G3_T = 64;
constexpr int K1_DUMMY = G1_STAGE * G1_NS;
constexpr int K1_LDS = K1_DUMMY + 1024;
static_assert(G3_STAGE * G3_NS <= K1_DUMMY, "");
constexpr int G1_PF = 8, G3_PF = 16;

struct G1Frags { bf16x8_t a[4], ah, b[6], bh; };

struct G1Base { unsigned a, ah, b0, b1, bh; };
template <int SO>
DI void g1_read(G1Frags& f, const G1Base& q) {
#pragma unroll
  for (int p = 0; p < 3; ++p) {
    f.b[2 * p] = lds_tr(q.b0 + (SO + p * 4096), 512);
    f.b[2 * p + 1] = lds_tr(q.b1 + (SO + p * 4096), 512);
  }
#pragma unroll
  for (int m = 0; m < 4; ++m) f.a[m] = lds_b128(q.a + (SO + m * 1024));
  f.bh = lds_tr(q.bh + SO, 512);
  f.ah = lds_b128(q.ah + SO);
}
DI void g1_mma(f32x4 (&acc)[4][6], f32x4& acch, const G1Frags& f) {
  acch = mfma(f.bh, f.ah, acch);
#pragma unroll
  for (int m = 0; m < 4; ++m)
#pragma unroll
    for (int n = 0; n < 6; ++n)
      acc[m][n] = n < 4 ? mfma(f.b[n], f.a[m], acc[m][n]) : mfma(f.a[m], f.b[n], acc[m][n]);
}
template <int NMFMA, int NDS>
DI void interleave() {
#pragma unroll
  for (int i = 0; i < NMFMA; ++i) {
    __builtin_amdgcn_sched_group_barrier(0x008, 1, 0);
    if (i < NDS) __builtin_amdgcn_sched_group_barrier(0x100, 1, 0);
    __builtin_amdgcn_sched_group_barrier(0x002, 1, 0);
  }
}

template <int NMFMA, int NDS>
DI void interleave2() {
#pragma unroll
  for (int i = 0; i < NMFMA; ++i) {
    __builtin_amdgcn_sched_group_barrier(0x008, 1, 0);
    if (2 * i < NDS) __builtin_amdgcn_sched_group_barrier(0x100, 2, 0);
    __builtin_amdgcn_sched_group_barrier(0x002, 1, 0);
  }
}

DI void gemm1_attn(int tile_m, int tile_n, const char* __restrict__ wqkv, char* __restrict__ ws, const char* pfb, unsigned pf0, unsigned pfn) {
  const int tid = threadIdx.x, lane = tid & 63;
  const int w = __builtin_amdgcn_readfirstlane(tid >> 6);
  const int wm = w >> 1, wn = w & 1;
  Stamps stp; stp.at(0);
  const char* srcA = ws + WS_XT;
  const char* srcB = wqkv + (size_t)tile_n * 128;
  const unsigned voffA = (unsigned)(tile_m * 16 + 2 * w) * 1024 + lane * 16;
  const unsigned voffH = (unsigned)(tile_m > 0 ? tile_m * 16 - 1 : 0) * 1024 + lane * 16;
  auto voffB = [&](int n) {
    const int r = (n & 3) * 8 + (lane >> 3), c = (lane & 7) ^ (sw_std(r) << 1);
    return (unsigned)(r * (3 * D_ * 2) + (n >> 2) * (D_ * 2) + c * 16);
  };
  const unsigned voffB0 = voffB(w);
  const unsigned voffX = (w < 4) ? voffB(8 + w) : voffH;
  const char* srcX = w < 4 ? srcB : srcA;
  const unsigned strX = w < 4 ? 32 * 3 * D_ * 2 : 128 * 1024, dstX = w < 4 ? 17 * 1024 + (8 + w) * 1024 : 16 * 1024;
  auto issue = [&](int st, unsigned slot) {
    const char* a = srcA + (size_t)st * (128 * 1024);
    const char* b = srcB + (size_t)st * (32 * 3 * D_ * 2);
    glds16(slot + 2 * w * 1024, voffA, a);
    glds16(slot + 2 * w * 1024 + 1024, voffA, a + 1024);
    glds16(slot + 17 * 1024 + w * 1024, voffB0, b);
    glds16(slot + dstX, voffX, srcX + (size_t)st * strX);
  };
  auto waitk = [&](int k) { wait_vm_k<4, G1_NS - 1>(k); };
  auto waitx = [&](int k) { wait_vm_k<4, G1_NS - 1, G1_PF>(k); };
  float gate = ((const float*)(ws + WS_G))[tile_m * 256 + wm * 64 + lane];
  asm volatile("" : "+v"(gate));
  const unsigned a_base = wm * 4096 + lane * 16;
  const unsigned ah_base = 16 * 1024 + lane * 16;
  const int trr = (lane >> 4) * 8 + ((lane & 15) >> 2), trp = lane & 3;
  auto tr_addr = [&](int jb) { return (unsigned)(17 * 1024 + trr * 128 + ((jb ^ sw_std(trr)) * 32) + trp * 8); };
  const unsigned b0 = tr_addr(2 * wn), b1 = tr_addr(2 * wn + 1);
  const unsigned bh = tr_addr(2 * wn + (wm & 1)) + (1 + (wm >> 1)) * 4096;

  f32x4 acc[4][6], acch = {0.f, 0.f, 0.f, 0.f};
#pragma unroll
  for (int m = 0; m < 4; ++m)
#pragma unroll
    for (int n = 0; n < 6; ++n) acc[m][n] = f32x4{0.f, 0.f, 0.f, 0.f};

#pragma unroll
  for (int s = 0; s < G1_NS; ++s) issue(s, s * G1_STAGE);
  waitk(G1_NS - 1);
  bar_raw();
  stp.at(1);
  G1Base qb[3];
#pragma unroll
  for (int i = 0; i < 3; ++i) {
    const unsigned o = i * 2 * G1_STAGE;
    qb[i] = G1Base{pin(a_base + o), pin(ah_base + o), pin(b0 + o), pin(b1 + o), pin(bh + o)};
  }
  G1Frags fx, fy;
  g1_read<0>(fx, qb[0]);
  auto step = [&](auto slc, auto tail, int t, G1Frags& fc, G1Frags& fn) {
    constexpr int SL = decltype(slc)::v, NX = (SL + 1) % G1_NS;
    if constexpr (decltype(tail)::v == 0) {
      waitk(G1_NS - 2);
      bar_lgkm();
      issue(t + G1_NS, SL * G1_STAGE);
    } else {
      if (t + G1_NS <= G1_T) waitk(G1_NS - 2);
      else waitx(G1_T - 2 - t);
      bar_lgkm();
      if (t + G1_NS < G1_T) issue(t + G1_NS, SL * G1_STAGE);
      else if (t + G1_NS == G1_T) warm<G1_PF>(pfb, pf0 + w * G1_PF, pfn, K1_DUMMY);
    }
    __builtin_amdgcn_sched_barrier(0);
    g1_read<(NX & 1) * G1_STAGE>(fn, qb[NX >> 1]);
    g1_mma(acc, acch, fc);
    interleave<25, 19>();
    __builtin_amdgcn_sched_barrier(0);
    asm volatile("" : "+v"(acch));
  };
  auto ten = [&](auto tail, int t) {
    step(IC<0>{}, tail, t, fx, fy);     step(IC<1>{}, tail, t + 1, fy, fx);
    step(IC<2>{}, tail, t + 2, fx, fy); step(IC<3>{}, tail, t + 3, fy, fx);
    step(IC<4>{}, tail, t + 4, fx, fy); step(IC<0>{}, tail, t + 5, fy, fx);
    step(IC<1>{}, tail, t + 6, fx, fy); step(IC<2>{}, tail, t + 7, fy, fx);
    step(IC<3>{}, tail, t + 8, fx, fy); step(IC<4>{}, tail, t + 9, fy, fx);
  };
  for (int t = 0; t < 20; t += 10) ten(IC<0>{}, t);
  ten(IC<1>{}, 20);
  step(IC<0>{}, IC<1>{}, 30, fx, fy);
  g1_mma(acc, acch, fy);
  bar_lgkm();
  stp.at(2);

  const int ln = pin(threadIdx.x) & 63, qi = ln & 15, g = ln >> 4;
  typedef __attribute__((address_space(3))) u32x2* l64;
  u32x2 pk[4][6];
#pragma unroll
  for (int m = 0; m < 4; ++m)
#pragma unroll
    for (int n = 0; n < 6; ++n) pk[m][n] = u32x2{pack2(acc[m][n][0], acc[m][n][1]), pack2(acc[m][n][2], acc[m][n][3])};
  const unsigned xb = wn * 8192;
  if (wm < 3) {
#pragma unroll
    for (int n = 2; n < 6; ++n) *(l64)(size_t)(xb + (wm + 1) * 2048 + (n - 2) * 512 + ln * 8) = pk[3][n];
  }
  *(l64)(size_t)(xb + wm * 512 + (wm < 2 ? ln * 8 : qi * 32 + g * 8)) = u32x2{pack2(acch[0], acch[1]), pack2(acch[2], acch[3])};
  bar_lgkm();
  u32x2 pv[4];
  {
    const unsigned ra = xb + wm * 2048 + ln * 8;
    pv[0] = *(l64)(size_t)ra;
    pv[1] = *(l64)(size_t)(ra + 512);
    if (wm == 0) {
      pv[2] = __builtin_bit_cast(u32x2, __builtin_amdgcn_ds_read_tr16_b64_v4i16((__attribute__((address_space(3))) s16x4*)(size_t)(ra + 1024)));
      pv[3] = __builtin_bit_cast(u32x2, __builtin_amdgcn_ds_read_tr16_b64_v4i16((__attribute__((address_space(3))) s16x4*)(size_t)(ra + 1536)));
    } else {
      pv[2] = *(l64)(size_t)(ra + 1024);
      pv[3] = *(l64)(size_t)(ra + 1536);
    }
  }
  auto frag = [](u32x2 a, u32x2 b) { return __builtin_bit_cast(bf16x8_t, u32x4{a[0], a[1], b[0], b[1]}); };
  auto fbits = [](unsigned u) { return __builtin_bit_cast(float, u); };
  auto ubits = [](float f) { return __builtin_bit_cast(unsigned, f); };
  const bool odd = qi & 1;
  const bool cur0 = 4 * g + (qi & 1) <= qi, cur1 = 4 * g + 2 + (qi & 1) <= qi;
  const rsrc_t ro = mkrs(ws + WS_OT, S_ * D_ * 2);
  const unsigned dst = ((tile_n * 2 + wn) * 128 + tile_m * 16 + wm * 4) * 1024 + ln * 16;
  const f32x4 z4 = {0.f, 0.f, 0.f, 0.f};
#pragma unroll
  for (int mi = 0; mi < 4; ++mi) {
    const int m = (mi + 1) & 3;
    const u32x2 k0 = m ? pk[m ? m - 1 : 0][2] : pv[0], k1 = m ? pk[m ? m - 1 : 0][3] : pv[1];
    const u32x2 v0 = m ? pk[m ? m - 1 : 0][4] : pv[2], v1 = m ? pk[m ? m - 1 : 0][5] : pv[3];
    const bf16x8_t q = frag(pk[m][0], pk[m][1]);
    const f32x4 sp = mfma(frag(k0, k1), q, z4), sc = mfma(frag(pk[m][2], pk[m][3]), q, z4);
    const float cs = 0.17677669529663687f * 1.4426950408889634f;
    float l0 = (cur0 ? (odd ? sc[1] : sc[0]) : (odd ? sp[1] : sp[0])) * cs;
    float l1 = (cur1 ? (odd ? sc[3] : sc[2]) : (odd ? sp[3] : sp[2])) * cs;
    if (m == 0 && tile_m == 0 && wm == 0) {
      l0 = cur0 ? l0 : -1e9f;
      l1 = cur1 ? l1 : -1e9f;
    }
    float mx = fmaxf(l0, l1);
    u32x2 t = __builtin_amdgcn_permlane16_swap(ubits(mx), ubits(mx), false, false);
    mx = fmaxf(fbits(t[0]), fbits(t[1]));
    t = __builtin_amdgcn_permlane32_swap(ubits(mx), ubits(mx), false, false);
    mx = fmaxf(fbits(t[0]), fbits(t[1]));
    const unsigned pp = pack2(__builtin_amdgcn_exp2f(l0 - mx), __builtin_amdgcn_exp2f(l1 - mx));
    float sum = bf_lo(pp) + bf_hi(pp);
    t = __builtin_amdgcn_permlane16_swap(ubits(sum), ubits(sum), false, false);
    sum = fbits(t[0]) + fbits(t[1]);
    t = __builtin_amdgcn_permlane32_swap(ubits(sum), ubits(sum), false, false);
    sum = fbits(t[0]) + fbits(t[1]);
    const unsigned t0 = odd ? pp << 16 : pp & 0xffffu, t1 = odd ? pp & 0xffff0000u : pp >> 16;
    const bf16x8_t pf = frag(u32x2{cur0 ? 0u : t0, cur1 ? 0u : t1}, u32x2{cur0 ? t0 : 0u, cur1 ? t1 : 0u});
    const f32x4 o0 = mfma(frag(v0, pk[m][4]), pf, z4), o1 = mfma(frag(v1, pk[m][5]), pf, z4);
    const float gs = fbits(__builtin_amdgcn_ds_bpermute((m * 16 + qi) * 4, ubits(gate))) * __builtin_amdgcn_rcpf(sum);
    st16<AUX1>(ro, dst + m * 1024,
               u32x4{pack2(o0[0] * gs, o0[1] * gs), pack2(o0[2] * gs, o0[3] * gs), pack2(o1[0] * gs, o1[1] * gs), pack2(o1[2] * gs, o1[3] * gs)});
  }
  wait_vm<0>();
  stp.at(3); stp.out(ws, 0);
}

struct G3Frags { bf16x8_t h[4], w[7]; };

DI unsigned gelu_tanh2(float u0, float u1) {
  const float c0 = -2.0f * 0.7978845608028654f * 1.4426950408889634f;
  const float c1 = c0 * 0.044715f;
  const f32x2 u = {u0, u1};
  const f32x2 a = u * __builtin_elementwise_fma(u * u, f32x2{c1, c1}, f32x2{c0, c0});
  const f32x2 d = f32x2{__builtin_amdgcn_exp2f(a[0]), __builtin_amdgcn_exp2f(a[1])} + f32x2{1.0f, 1.0f};
  const f32x2 r = u * f32x2{__builtin_amdgcn_rcpf(d[0]), __builtin_amdgcn_rcpf(d[1])};
  return __builtin_bit_cast(unsigned, __builtin_convertvector(r, bf16x2_t));
}

DI void gemm3_gelu(int tile_d, int tile_f, const char* __restrict__ w1, const char* __restrict__ b1, char* __restrict__ ws, const char* pfb, unsigned pf0, unsigned pfn) {
  const int tid = threadIdx.x, lane = tid & 63;
  const int w = __builtin_amdgcn_readfirstlane(tid >> 6);
  const int odd = tile_f & 1;
  Stamps stp; stp.at(0);
  if (w >= 4) {
    const int lw = w - 4;
    const char* srcA = ws + WS_HT;
    const char* srcB = w1 + (size_t)tile_f * 448;
    const unsigned voffA = (unsigned)(tile_d * 8 + 2 * lw) * 1024 + lane * 16;
    const int r = lw * 8 + (lane >> 3), c = (lane & 7) ^ (sw_std(r) << 1);
    const unsigned voffB = (unsigned)(r * (F_ * 2) + odd * 64 + c * 16);
    const int hg = (lw & 1) * 2 + ((lane >> 4) & 1), hr = hg * 8 + (lane >> 5) * 4 + ((lane >> 2) & 3);
    const int hc = (lane & 3) ^ ((hg & 1) << 1);
    const unsigned voffH = (unsigned)(hr * (F_ * 2) + (odd ? 0 : 384) + hc * 16);
    auto issue = [&](int st, unsigned slot) {
      const char* a = srcA + (size_t)st * (64 * 1024);
      const char* b = srcB + (size_t)st * (32 * F_ * 2);
      glds16(slot + 2 * lw * 1024, voffA, a);
      glds16(slot + 2 * lw * 1024 + 1024, voffA + 1024, a);
      glds16(slot + 8192 + lw * 1024, voffB, b);
      glds16(slot + 8192 + 4096 + lw * 1024, voffB + 128, b);
      glds16(slot + 8192 + 8192 + lw * 1024, voffB + 256, b);
      if (lw < 2) glds16(slot + 8192 + 12288 + lw * 1024, voffH, b);
    };
    auto waitk = [&](int k) {
      if (lw < 2) wait_vm_k<6, G3_NS - 1>(k);
      else wait_vm_k<5, G3_NS - 1>(k);
    };
    auto waitx = [&](int k) {
      if (lw < 2) wait_vm_k<6, G3_NS - 1, G3_PF>(k);
      else wait_vm_k<5, G3_NS - 1, G3_PF>(k);
    };
#pragma unroll
    for (int s = 0; s < G3_NS; ++s) issue(s, s * G3_STAGE);
    waitk(G3_NS - 1);
    bar_raw();
    unsigned cur = 0;
    for (int t = 0; t < G3_T - G3_NS; ++t) {
      waitk(G3_NS - 2);
      bar_raw();
      issue(t + G3_NS, cur);
      cur += G3_STAGE;
      if (cur == G3_NS * G3_STAGE) cur = 0;
    }
    waitk(G3_NS - 2);
    bar_raw();
    warm<G3_PF>(pfb, pf0 + lw * G3_PF, pfn, K1_DUMMY);
#pragma unroll
    for (int k = G3_NS - 3; k >= 0; --k) {
      waitx(k);
      bar_raw();
    }
    wait_vm<0>();
    return;
  }
  const int wd = w >> 1, wf = w & 1;
  const unsigned h_base = wd * 4096 + lane * 16;
  const int g = lane >> 4, qq = (lane & 15) >> 2, trp = lane & 3, trr = g * 8 + qq;
  unsigned wb[7];
  int fcol[7];
#pragma unroll
  for (int i = 0; i < 7; ++i) {
    const int lt = wf * 7 + i;
    const unsigned pa = 8192 + (lt >> 2) * 4096 + trr * 128 + (((lt & 3) ^ sw_std(trr)) * 32) + trp * 8;
    const unsigned ha = 8192 + 12288 + (g >> 1) * 1024 + (g & 1) * 256 + qq * 64 + (((lt & 1) ^ (g & 1)) * 32) + trp * 8;
    wb[i] = lt < 12 ? pa : ha;
    fcol[i] = tile_f * 224 + (odd ? (lt < 12 ? 32 + 16 * lt : 16 * (lt - 12)) : 16 * lt);
  }
  f32x4 acc[7][4];
#pragma unroll
  for (int i = 0; i < 7; ++i) {
    const u32x2 bias = *(const u32x2*)(b1 + (size_t)(fcol[i] + 4 * g) * 2);
    const unsigned b0 = bias[0], b1v = bias[1];
#pragma unroll
    for (int m = 0; m < 4; ++m) acc[i][m] = f32x4{bf_lo(b0), bf_hi(b0), bf_lo(b1v), bf_hi(b1v)};
  }
  unsigned hq[2], wq[2][7];
#pragma unroll
  for (int c = 0; c < 2; ++c) {
    hq[c] = pin(h_base + c * 3 * G3_STAGE);
#pragma unroll
    for (int i = 0; i < 7; ++i) wq[c][i] = pin(wb[i] + c * 3 * G3_STAGE);
  }
  auto rd = [&](G3Frags& f, auto slc) {
    constexpr int C = decltype(slc)::v / 3, SO = (decltype(slc)::v % 3) * G3_STAGE;
#pragma unroll
    for (int m = 0; m < 4; ++m) f.h[m] = lds_b128(hq[C] + (SO + m * 1024));
#pragma unroll
    for (int i = 0; i < 7; ++i) f.w[i] = lds_tr(wq[C][i] + SO, 512);
  };
  auto mm = [&](const G3Frags& f) {
#pragma unroll
    for (int i = 0; i < 7; ++i)
#pragma unroll
      for (int m = 0; m < 4; ++m) acc[i][m] = mfma(f.w[i], f.h[m], acc[i][m]);
  };
  G3Frags fx, fy;
  bar_raw();
  stp.at(1);
  rd(fx, IC<0>{});
  auto step = [&](auto nx, G3Frags& fc, G3Frags& fn) {
    bar_lgkm();
    __builtin_amdgcn_sched_barrier(0);
    rd(fn, nx);
    mm(fc);
    interleave<28, 18>();
    __builtin_amdgcn_sched_barrier(0);
  };
  for (int t = 0; t < 60; t += 6) {
    step(IC<1>{}, fx, fy); step(IC<2>{}, fy, fx); step(IC<3>{}, fx, fy);
    step(IC<4>{}, fy, fx); step(IC<5>{}, fx, fy); step(IC<0>{}, fy, fx);
  }
  step(IC<1>{}, fx, fy); step(IC<2>{}, fy, fx); step(IC<3>{}, fx, fy);
  mm(fy);
  stp.at(2);
  const rsrc_t ru = mkrs(ws + WS_UT, D_ * F_ * 2);
  const int ln = pin(threadIdx.x) & 63;
#pragma unroll
  for (int m = 0; m < 4; ++m) {
    u32x2 pk[7];
#pragma unroll
    for (int i = 0; i < 7; ++i) {
      pk[i] = u32x2{gelu_tanh2(acc[i][m][0], acc[i][m][1]), gelu_tanh2(acc[i][m][2], acc[i][m][3])};
    }
    const unsigned rbo = (unsigned)(tile_d * 8 + wd * 4 + m) * 1024 + ln * 16;
    if (wf == 0) {
#pragma unroll
      for (int p = 0; p < 3; ++p)
        st16<AUX1>(ru, (fcol[2 * p] >> 5) * 65536 + rbo, u32x4{pk[2 * p][0], pk[2 * p][1], pk[2 * p + 1][0], pk[2 * p + 1][1]});
      st8<AUX1H>(ru, (fcol[6] >> 5) * 65536 + rbo, pk[6]);
    } else {
      st8<AUX1H>(ru, (fcol[0] >> 5) * 65536 + rbo + 8, pk[0]);
#pragma unroll
      for (int p = 0; p < 3; ++p)
        st16<AUX1>(ru, (fcol[2 * p + 1] >> 5) * 65536 + rbo,
                   u32x4{pk[2 * p + 1][0], pk[2 * p + 1][1], pk[2 * p + 2][0], pk[2 * p + 2][1]});
    }
  }
  stp.at(3); stp.out(ws, 0);
}

__global__ __launch_bounds__(512, 2) void k1_gemms(const char* __restrict__ wqkv, const char* __restrict__ w1,
                                                   const char* __restrict__ b1, char* __restrict__ ws,
                                                   const char* __restrict__ w2, const char* __restrict__ wo) {
  const int b = blockIdx.x, xcd = b & 7, j = b >> 3;
  const char* pfb = b < 224 ? w2 : wo;
  const unsigned pf0 = (b < 224 ? b : b - 224) * 64, pfn = b < 224 ? F_ * S_ * 2 / 1024 : D_ * D_ * 2 / 1024;
  if (xcd < 4) gemm1_attn((xcd & 1) * 4 + (j & 3), (xcd >> 1) * 8 + (j >> 2), wqkv, ws, pfb, pf0, pfn);
  else gemm3_gelu(j & 7, (xcd - 4) * 4 + (j >> 3), w1, b1, ws, pfb, pf0, pfn);
}

#define K2_NA 34
#define K2_POLL 14
#define K2_LOAD 15
constexpr int K2_STAGE = 32 * 1024, K2_NS = 5, K2_T1 = 56, K2_T = 72;
constexpr int K2_LDS = K2_STAGE * K2_NS;

struct K2Frags { bf16x8_t s[4], d[4]; };
struct K2Base { unsigned t[3][4], l[3]; };
template <int B, int E, class F>
DI void sfor(F&& f) {
  if constexpr (B < E) { f(IC<B>{}); sfor<B + 1, E>(f); }
}

template <bool RECV>
DI void k2_consumer(int tile_s, int tile_d, int w, const char* __restrict__ b2, char* __restrict__ ws, char* __restrict__ out) {
  const int lane = threadIdx.x & 63;
  Stamps stp; stp.at(0);
  const int ws_ = w >> 1, wd = w & 1;
  const int g = lane >> 4, trr = g * 4 + ((lane & 15) >> 2), trp = lane & 3;
  float gt[4], bb[4];
#pragma unroll
  for (int i = 0; i < 4; ++i) {
    const int s = tile_s * 128 + ws_ * 64 + i * 16 + (lane & 15);
    gt[i] = 1.0f - ((const float*)(ws + WS_G))[s];
    bb[i] = bf_lo(((const unsigned short*)b2)[s]);
  }
  f32x4 acc[4][4];
#pragma unroll
  for (int i = 0; i < 4; ++i)
#pragma unroll
    for (int n = 0; n < 4; ++n) acc[i][n] = f32x4{0.f, 0.f, 0.f, 0.f};
  K2Base q1, q2;
  auto mkbase = [&](K2Base& q, unsigned toff, unsigned loff) {
#pragma unroll
    for (int c = 0; c < 3; ++c) {
      q.l[c] = pin(lane * 16 + loff + c * 65536);
#pragma unroll
      for (int i = 0; i < 4; ++i) q.t[c][i] = pin(trr * 128 + ((i ^ sw_prm(trr)) * 32) + trp * 8 + toff + c * 65536);
    }
  };
  mkbase(q1, ws_ * 4096, 16384 + wd * 4096);
  auto rd = [&](K2Frags& f, auto part, auto slot, auto kkc) {
    constexpr int C = decltype(slot)::v >> 1, SO = (decltype(slot)::v & 1) * K2_STAGE + decltype(kkc)::v * 8192;
    if constexpr (decltype(part)::v == 1) {
#pragma unroll
      for (int i = 0; i < 4; ++i) {
        f.d[i] = lds_b128(q1.l[C] + (SO + i * 1024));
        f.s[i] = lds_tr(q1.t[C][i] + SO, 2048);
      }
    } else {
#pragma unroll
      for (int i = 0; i < 4; ++i) {
        f.s[i] = lds_b128(q2.l[C] + (SO + i * 1024));
        f.d[i] = lds_tr(q2.t[C][i] + SO, 2048);
      }
    }
  };
  auto mm = [&](const K2Frags& f) {
#pragma unroll
    for (int i = 0; i < 4; ++i)
#pragma unroll
      for (int n = 0; n < 4; ++n) acc[i][n] = mfma(f.d[n], f.s[i], acc[i][n]);
  };
#pragma unroll
  for (int i = 0; i < 4; ++i) asm volatile("" : "+v"(gt[i]), "+v"(bb[i]));
  K2Frags fx, fy;
  bar_raw();
  stp.at(1);
  rd(fx, IC<1>{}, IC<0>{}, IC<0>{});
  auto stage = [&](auto slot, auto pc, auto pn) {
    constexpr int SL = decltype(slot)::v % K2_NS;
    rd(fy, pc, IC<SL>{}, IC<1>{});
    mm(fx);
    interleave2<16, 12>();
    __builtin_amdgcn_sched_barrier(0);
    if constexpr (decltype(pn)::v != 0) {
      bar_lgkm();
      __builtin_amdgcn_sched_barrier(0);
      rd(fx, pn, IC<(SL + 1) % K2_NS>{}, IC<0>{});
    }
    mm(fy);
    interleave2<16, 12>();
    __builtin_amdgcn_sched_barrier(0);
  };
  constexpr int N1 = K2_T1 - K2_NA, ITS = (K2_NA - 1) / 5, ITR = (N1 - 1) / 5, TS = K2_NA - 5 * ITS, TR = N1 - 5 * ITR;
  for (int it = RECV ? ITR : ITS; it > 0; --it) sfor<0, 5>([&](auto i) { stage(i, IC<1>{}, IC<1>{}); });
  const int unit = (tile_s * 8 + tile_d) * 4 + w;
  const __amdgpu_buffer_rsrc_t rsP = __builtin_amdgcn_make_buffer_rsrc((void*)(ws + WS_P + (size_t)unit * 8192), 0, 8192, 0x00020000);
  unsigned* flag = (unsigned*)(ws + WS_FLAG + (size_t)unit * 128);
  if constexpr (!RECV) {
    sfor<0, TS - 1>([&](auto i) { stage(i, IC<1>{}, IC<1>{}); });
    stage(IC<TS - 1>{}, IC<1>{}, IC<0>{});
    stp.at(2);
    const int ln = pin(threadIdx.x) & 63;
#pragma unroll
    for (int i = 0; i < 4; ++i)
#pragma unroll
      for (int n = 0; n < 4; n += 2) {
        const f32x4 v = acc[i][n] * gt[i], u = acc[i][n + 1] * gt[i];
        st16<16>(rsP, ln * 16 + (i * 2 + n / 2) * 1024, u32x4{pack2(v[0], v[1]), pack2(v[2], v[3]), pack2(u[0], u[1]), pack2(u[2], u[3])});
      }
    wait_vm<0>();
    if (ln == 0) __hip_atomic_store(flag, 1u, __ATOMIC_RELAXED, __HIP_MEMORY_SCOPE_AGENT);
    stp.at(3); stp.out(ws, 1);
  } else {
  sfor<0, TR - 1>([&](auto i) { stage(i, IC<1>{}, IC<1>{}); });
  mkbase(q2, 16384 + wd * 4096, ws_ * 4096);
  stage(IC<TR - 1>{}, IC<1>{}, IC<2>{});
#pragma unroll
  for (int i = 0; i < 4; ++i)
#pragma unroll
    for (int n = 0; n < 4; ++n)
#pragma unroll
      for (int e = 0; e < 4; ++e) acc[i][n][e] = (acc[i][n][e] + bb[i]) * gt[i];
  __builtin_amdgcn_sched_barrier(0);
  for (int it = 0; it < 2; ++it) sfor<TR, TR + 5>([&](auto i) { stage(i, IC<2>{}, IC<2>{}); });
  sfor<TR + 10, TR + K2_POLL>([&](auto i) { stage(i, IC<2>{}, IC<2>{}); });
  unsigned fl = __hip_atomic_load(flag, __ATOMIC_RELAXED, __HIP_MEMORY_SCOPE_AGENT);
  __builtin_amdgcn_sched_barrier(0);
  sfor<TR + K2_POLL, TR + K2_LOAD>([&](auto i) { stage(i, IC<2>{}, IC<(decltype(i)::v == TR + 15 ? 0 : 2)>{}); });
#pragma unroll
  for (int i = 0; i < 4; ++i) asm volatile("" : "+v"(acc[i][0]), "+v"(acc[i][1]), "+v"(acc[i][2]), "+v"(acc[i][3]));
  for (int spin = 0; __builtin_amdgcn_readfirstlane(fl) == 0 && spin < (1 << 20); ++spin) {
    __builtin_amdgcn_s_sleep(1);
    fl = __hip_atomic_load(flag, __ATOMIC_RELAXED, __HIP_MEMORY_SCOPE_AGENT);
  }
  asm volatile("" ::: "memory");
  const int ln = pin(threadIdx.x) & 63;
  u32x4 part[4][2];
#pragma unroll
  for (int i = 0; i < 4; ++i)
#pragma unroll
    for (int n = 0; n < 2; ++n) part[i][n] = __builtin_amdgcn_raw_buffer_load_b128(rsP, ln * 16 + (i * 2 + n) * 1024, 0, 16);
  __builtin_amdgcn_sched_barrier(0);
  sfor<TR + K2_LOAD, TR + 16>([&](auto i) { stage(i, IC<2>{}, IC<(decltype(i)::v == TR + 15 ? 0 : 2)>{}); });
  stp.at(2);
  const rsrc_t rout = mkrs(out, S_ * D_ * 2);
  const unsigned oo = ((tile_s * 128 + ws_ * 64 + (ln & 15)) * D_ + tile_d * 128 + wd * 64 + ((ln >> 4) & 1) * 16 + (ln >> 5) * 8) * 2;
#pragma unroll
  for (int i = 0; i < 4; ++i)
#pragma unroll
    for (int n = 0; n < 4; n += 2) {
      const unsigned p0 = part[i][n / 2][0], p1 = part[i][n / 2][1], p2 = part[i][n / 2][2], p3 = part[i][n / 2][3];
      const f32x4 v = acc[i][n] + f32x4{bf_lo(p0), bf_hi(p0), bf_lo(p1), bf_hi(p1)};
      const f32x4 u = acc[i][n + 1] + f32x4{bf_lo(p2), bf_hi(p2), bf_lo(p3), bf_hi(p3)};
      const u32x2 lo = __builtin_amdgcn_permlane16_swap(pack2(v[0], v[1]), pack2(u[0], u[1]), false, false);
      const u32x2 hi = __builtin_amdgcn_permlane16_swap(pack2(v[2], v[3]), pack2(u[2], u[3]), false, false);
      st16<AUX2>(rout, oo + (i * 16 * D_ + n * 16) * 2, u32x4{lo[0], hi[0], lo[1], hi[1]});
    }
  stp.at(3); stp.out(ws, 1);
  }
}

__global__ __launch_bounds__(512, 2) void k2_out(const char* __restrict__ wo, const char* __restrict__ w2,
                                                 const char* __restrict__ b2, char* __restrict__ ws,
                                                 char* __restrict__ out) {
  const int b = blockIdx.x, xcd = b & 7, j = b >> 3;
  const int recv = xcd & 1, quad = xcd >> 1;
  const int tile_s = (quad & 1) * 8 + (j & 7), tile_d = (quad >> 1) * 4 + (j >> 3);
  const int tid = threadIdx.x, lane = tid & 63;
  const int w = __builtin_amdgcn_readfirstlane(tid >> 6);
  if (w >= 4) {
    const int lw = w - 4;
    const int st0 = recv ? K2_NA : 0, nst = recv ? K2_T - K2_NA : K2_NA;
    const int r = lw * 8 + (lane >> 3), c = (lane & 7) ^ (sw_prm(r) << 1);
    const unsigned voffW2 = (unsigned)(r * (S_ * 2) + tile_s * 256 + c * 16);
    const unsigned voffU = (unsigned)(tile_d * 8 + 2 * lw) * 1024 + lane * 16;
    const unsigned voffO = (unsigned)(tile_s * 8 + 2 * lw) * 1024 + lane * 16;
    const unsigned voffWo = (unsigned)(r * (D_ * 2) + tile_d * 256 + c * 16);
    auto issue = [&](int st, unsigned slot) {
      if (st < K2_T1) {
        const char* a = w2 + (size_t)st * (64 * S_ * 2);
        const char* u = ws + WS_UT + (size_t)st * (2 * 65536);
#pragma unroll
        for (int kk = 0; kk < 2; ++kk) {
          glds16(slot + kk * 8192 + lw * 1024, voffW2 + kk * (32 * S_ * 2), a);
          glds16(slot + kk * 8192 + 4096 + lw * 1024, voffW2 + kk * (32 * S_ * 2) + 128, a);
          glds16(slot + 16384 + kk * 8192 + 2 * lw * 1024, voffU + kk * 65536, u);
          glds16(slot + 16384 + kk * 8192 + 2 * lw * 1024 + 1024, voffU + kk * 65536 + 1024, u);
        }
      } else {
        const char* o = ws + WS_OT + (size_t)(st - K2_T1) * (2 * 131072);
        const char* a = wo + (size_t)(st - K2_T1) * (64 * D_ * 2);
#pragma unroll
        for (int kk = 0; kk < 2; ++kk) {
          glds16(slot + kk * 8192 + 2 * lw * 1024, voffO + kk * 131072, o);
          glds16(slot + kk * 8192 + 2 * lw * 1024 + 1024, voffO + kk * 131072 + 1024, o);
          glds16(slot + 16384 + kk * 8192 + lw * 1024, voffWo + kk * (32 * D_ * 2), a);
          glds16(slot + 16384 + kk * 8192 + 4096 + lw * 1024, voffWo + kk * (32 * D_ * 2) + 128, a);
        }
      }
    };
#pragma unroll
    for (int s = 0; s < K2_NS; ++s) issue(st0 + s, s * K2_STAGE);
    wait_vm<8 * (K2_NS - 1)>();
    bar_raw();
    unsigned cur = 0;
    for (int t = 0; t < nst - 1; ++t) {
      wait_vm_k<8, K2_NS - 2>(min(K2_NS - 2, nst - 2 - t));
      bar_raw();
      if (t + K2_NS < nst) issue(st0 + t + K2_NS, cur);
      cur += K2_STAGE;
      if (cur == K2_NS * K2_STAGE) cur = 0;
    }
    return;
  }
  if (recv) k2_consumer<true>(tile_s, tile_d, w, b2, ws, out);
  else k2_consumer<false>(tile_s, tile_d, w, b2, ws, out);
}

extern "C" void kernel_launch(void* const* d_in, const int* in_sizes, int n_in, void* d_out, int out_size, void* d_ws,
                              size_t ws_size, hipStream_t stream) {
  const char* x = (const char*)d_in[0];
  const char* wqkv = (const char*)d_in[1];
  const char* wo = (const char*)d_in[2];
  const char* w1 = (const char*)d_in[4];
  const char* b1 = (const char*)d_in[5];
  const char* w2 = (const char*)d_in[6];
  const char* b2 = (const char*)d_in[7];
  const char* gw = (const char*)d_in[8];
  char* ws = (char*)d_ws;
  (void)hipFuncSetAttribute((const void*)k1_gemms, hipFuncAttributeMaxDynamicSharedMemorySize, K1_LDS);
  (void)hipFuncSetAttribute((const void*)k2_out, hipFuncAttributeMaxDynamicSharedMemorySize, K2_LDS);
  k0_prep<<<256, 512, 0, stream>>>(x, gw, ws, w1);
  k1_gemms<<<256, 512, K1_LDS, stream>>>(wqkv, w1, b1, ws, w2, wo);
  k2_out<<<256, 512, K2_LDS, stream>>>(wo, w2, b2, ws, (char*)d_out);
}
```
